```python
import torch

def _softmax(x, axis=-1):
    return torch.softmax(x, dim=axis)
def _gelu(x):
    return torch.nn.functional.gelu(x, approximate="tanh")
def _silu(x):
    return torch.nn.functional.silu(x)
def _sigmoid(x):
    return torch.sigmoid(x)
def _layernorm(x, axis=-1, eps=1e-5):
    m = x.mean(dim=axis, keepdim=True)
    v = x.var(dim=axis, keepdim=True, unbiased=False)
    return (x - m) / torch.sqrt(v + eps)
def _scatter_add(tgt, idx, src, axis=0):
    return tgt.clone().index_add_(axis, idx, src)
def _segment_sum(src, seg, n):
    out = torch.zeros((n,) + src.shape[1:], dtype=src.dtype)
    return out.index_add_(0, seg, src)
def _pad(x, pw):
    flat = tuple(v for pair in reversed(pw) for v in pair)
    return torch.nn.functional.pad(x, flat)

kernel_name = 'gated_mix__mha_block_cached__glu_gated_geglu__decode'

def setup_inputs():

    _w = (lambda a: (lambda t: t.to(torch.bfloat16) if t.is_floating_point() else t)(torch.as_tensor(a)))
    def _A_setup():
        B, Sq, D = 8, 1, 576
        H, Sk, Dh = 32, 4096, 128
        return {
            "x": _w((lambda *s: torch.randn(*s, dtype=torch.bfloat16) * 0.05)(B, Sq, D)),
            "k_cache": _w((lambda *s: torch.randn(*s, dtype=torch.bfloat16) * 3.2)(B, H, Sk, Dh)),
            "v_cache": _w((lambda *s: torch.randn(*s, dtype=torch.bfloat16) * 0.05)(B, H, Sk, Dh)),
            "wq": _w((lambda *s: torch.randn(*s, dtype=torch.bfloat16) * 3.2)(D, H * Dh) * (D ** -0.5)),
            "wo": _w((lambda *s: torch.randn(*s, dtype=torch.bfloat16) * 0.05)(H * Dh, D) * ((H * Dh) ** -0.5)),
        }
    _A_in = _A_setup()

    def _B_setup():
        B, S, Dm, Df = 8, 1, 576, 4608
        return {
            "x": _w((lambda *s: torch.randn(*s, dtype=torch.bfloat16) * 0.05)(B, S, Dm)),
            "w_gate": _w((lambda *s: torch.randn(*s, dtype=torch.bfloat16) * 0.2)(Dm, Df) * (Dm ** -0.5)),
            "w_up": _w((lambda *s: torch.randn(*s, dtype=torch.bfloat16) * 0.2)(Dm, Df) * (Dm ** -0.5)),
            "w_down": _w((lambda *s: torch.randn(*s, dtype=torch.bfloat16) * 0.2)(Df, Dm) * (Df ** -0.5)),
        }
    _B_in = _B_setup()

    return {
        "A_x": _A_in["x"],
        "A_k_cache": _A_in["k_cache"],
        "A_v_cache": _A_in["v_cache"],
        "A_wq": _A_in["wq"],
        "A_wo": _A_in["wo"],
        "B_x": _B_in["x"],
        "B_w_gate": _B_in["w_gate"],
        "B_w_up": _B_in["w_up"],
        "B_w_down": _B_in["w_down"],
        "gate_w": _w((lambda *s: torch.randn(*s, dtype=torch.bfloat16) * 0.05)(576) * 16.0),
    }

def reference(A_x, A_k_cache, A_v_cache, A_wq, A_wo, B_x, B_w_gate, B_w_up, B_w_down, gate_w):
    def _A_ref(x, k_cache, v_cache, wq, wo):
        B, Sq, D = x.shape
        H, Dh = 32, 128
        q = (lambda x, *d: x.permute(*d))((x @ wq).reshape(B, Sq, H, Dh), 0, 2, 1, 3)
        scores = torch.einsum("bhqd,bhkd->bhqk", q, k_cache) * (Dh ** -0.5)
        p = _softmax(scores, axis=-1)
        o = torch.einsum("bhqk,bhkd->bhqd", p, v_cache)
        return (lambda x, *d: x.permute(*d))(o, 0, 2, 1, 3).reshape(B, Sq, H * Dh) @ wo

    def _B_ref(x, w_gate, w_up, w_down):
        g = torch.einsum("bsd,df->bsf", x, w_gate)
        u = torch.einsum("bsd,df->bsf", x, w_up)
        h = _gelu(g) * u
        return torch.einsum("bsf,fd->bsd", h, w_down)

    x = A_x
    g = _sigmoid((x * gate_w).sum(-1, keepdims=True))
    a = _A_ref(x, A_k_cache, A_v_cache, A_wq, A_wo)
    b = _B_ref(x, B_w_gate, B_w_up, B_w_down)
    return g * a + (1 - g) * b
```

```cpp
#include <hip/hip_runtime.h>
#include <hip/hip_bf16.h>

#define GAS __attribute__((address_space(1)))
#define LAS __attribute__((address_space(3)))
typedef unsigned u32x4 __attribute__((ext_vector_type(4)));
typedef unsigned u32x2 __attribute__((ext_vector_type(2)));
typedef float f32x4 __attribute__((ext_vector_type(4)));
typedef float f32x2 __attribute__((ext_vector_type(2)));
typedef __bf16 bf16x2 __attribute__((ext_vector_type(2)));
typedef unsigned short u16;
typedef unsigned long long u64;

#define NA 3
#define GT 4
#define NS 9
#define NTHREADS ((NS + NA) * 64)

constexpr int DM = 576, NH = 32, SK = 4096, DH = 128, DF = 4608;
constexpr unsigned TAG_STEP = 0x9E3779B9u;
constexpr unsigned SPIN_CAP = 1u << 18;

__device__ __forceinline__ float dot2(unsigned a, unsigned b, float c) {
  return __builtin_amdgcn_fdot2_f32_bf16(__builtin_bit_cast(bf16x2, a), __builtin_bit_cast(bf16x2, b), c, false);
}
__device__ __forceinline__ float bflo(unsigned v) { return __builtin_bit_cast(float, v << 16); }
__device__ __forceinline__ float bfhi(unsigned v) { return __builtin_bit_cast(float, v & 0xffff0000u); }
__device__ __forceinline__ unsigned pk_lo(unsigned a, unsigned b) { return __builtin_amdgcn_perm(b, a, 0x05040100u); }
__device__ __forceinline__ unsigned pk_hi(unsigned a, unsigned b) { return __builtin_amdgcn_perm(b, a, 0x07060302u); }
__device__ __forceinline__ unsigned pack2(float a, float b) {
  return __builtin_bit_cast(unsigned, __builtin_convertvector(f32x2{a, b}, bf16x2));
}
__device__ __forceinline__ unsigned f2bf(float f) { return pack2(f, 0.f) & 0xffffu; }
template <int CTRL> __device__ __forceinline__ float dpp(float v) {
  return __builtin_bit_cast(float, __builtin_amdgcn_update_dpp(0, __builtin_bit_cast(int, v), CTRL, 0xf, 0xf, true));
}
__device__ __forceinline__ float row_sum(float v) {
  v += dpp<0x128>(v); v += dpp<0x124>(v); v += dpp<0x122>(v); v += dpp<0x121>(v);
  return v;
}
__device__ __forceinline__ float xor16(float v) { return __builtin_bit_cast(float, __builtin_amdgcn_ds_bpermute((int)(((threadIdx.x & 63) ^ 16) << 2), __builtin_bit_cast(int, v))); }
__device__ __forceinline__ float xor32(float v) { return __builtin_bit_cast(float, __builtin_amdgcn_ds_bpermute((int)(((threadIdx.x & 63) ^ 32) << 2), __builtin_bit_cast(int, v))); }
__device__ __forceinline__ void wg_barrier() {
  asm volatile("s_waitcnt lgkmcnt(0)" ::: "memory");
  __builtin_amdgcn_s_barrier();
  asm volatile("" ::: "memory");
}
typedef __amdgpu_buffer_rsrc_t rsrc_t;
__device__ __forceinline__ rsrc_t mk_rsrc(const void* p, unsigned bytes) { return __builtin_amdgcn_make_buffer_rsrc((void*)p, (short)0, (int)bytes, 0x00020000); }
#define W_AUX 0
__device__ __forceinline__ u32x4 ldw(rsrc_t r, unsigned voff, unsigned soff) { return __builtin_amdgcn_raw_buffer_load_b128(r, (int)voff, (int)soff, W_AUX); }
__device__ __forceinline__ u32x4 ldnt(rsrc_t r, unsigned voff, unsigned soff) { return __builtin_amdgcn_raw_buffer_load_b128(r, (int)voff, (int)soff, 2); }

__device__ __forceinline__ void gemv_pair(float (&acc)[8], const u32x4& A, const u32x4& B, unsigned xp) {
#pragma unroll
  for (int d = 0; d < 4; ++d) {
    acc[2 * d] = dot2(pk_lo(A[d], B[d]), xp, acc[2 * d]);
    acc[2 * d + 1] = dot2(pk_hi(A[d], B[d]), xp, acc[2 * d + 1]);
  }
}

struct __attribute__((aligned(16))) Smem {
  float qpart[NS][DH];
  float gpart[16];
  float gu[2][32][144];
  float hs[144];
  float opart[NA][DH];
  float ml[8][2];
  float fin[8][DM];
  float gath[32][18];
  unsigned xs[DM / 2];
  unsigned ctr[4];
};

__global__ void __launch_bounds__(NTHREADS)
fused_decode_kernel(const u16* __restrict__ x, const u32x4* __restrict__ Kc, const u32x4* __restrict__ Vc,
                    const u16* __restrict__ wq, const u16* __restrict__ wo, const u16* __restrict__ wg,
                    const u16* __restrict__ wu, const u16* __restrict__ wd, const u16* __restrict__ gw,
                    u16* __restrict__ out, u64* slots, unsigned* epoch) {
  __shared__ Smem sm;
  const int t = threadIdx.x, lane = t & 63;
  const int wave = __builtin_amdgcn_readfirstlane(t >> 6);
  const int bid = blockIdx.x;
  const int h = 4 * (bid & 7) + ((bid >> 3) & 3);
  const int b = bid >> 5;

  if (wave >= NS) {
    __builtin_amdgcn_s_setprio(2);
    const int w = wave - NS;
    const size_t base = (size_t)(b * NH + h) * (SK * DH / 8);
    const rsrc_t kR = mk_rsrc(Kc + base, SK * DH * 2), vR = mk_rsrc(Vc + base, SK * DH * 2);
    const unsigned voff = lane * 16;
    unsigned soff = w * (GT * 1024);
    constexpr int NT = SK / (4 * GT);
    const int my_tiles = (NT - w + NA - 1) / NA;
    u32x4 kr[GT], vr[GT];
#pragma unroll
    for (int j = 0; j < GT; ++j) kr[j] = ldnt(kR, voff + j * 1024, soff);
#pragma unroll
    for (int j = 0; j < GT; ++j) vr[j] = ldnt(vR, voff + j * 1024, soff);
    wg_barrier();
    unsigned qh[4], ql[4];
    {
      const int c = lane & 15;
      f32x4 a = {0.f, 0.f, 0.f, 0.f}, bb = {0.f, 0.f, 0.f, 0.f};
#pragma unroll
      for (int s = 0; s < NS; ++s) {
        a += *(const f32x4*)&sm.qpart[s][8 * c];
        bb += *(const f32x4*)&sm.qpart[s][8 * c + 4];
      }
      const float sc = 0.08838834764831845f * 1.4426950408889634f;
      float q[8] = {a[0] * sc, a[1] * sc, a[2] * sc, a[3] * sc, bb[0] * sc, bb[1] * sc, bb[2] * sc, bb[3] * sc};
#pragma unroll
      for (int d = 0; d < 4; ++d) {
        const unsigned hi = pack2(q[2 * d], q[2 * d + 1]);
        qh[d] = hi;
        ql[d] = pack2(q[2 * d] - bflo(hi), q[2 * d + 1] - bfhi(hi));
      }
    }
    float m = -INFINITY, l = 0.f;
    float acc[8];
#pragma unroll
    for (int e = 0; e < 8; ++e) acc[e] = 0.f;

    auto tile = [&](auto refill) {
      float s[GT];
#pragma unroll
      for (int j = 0; j < GT; ++j) {
        float d0 = dot2(kr[j][0], qh[0], 0.f), d1 = dot2(kr[j][0], ql[0], 0.f);
#pragma unroll
        for (int d = 1; d < 4; ++d) { d0 = dot2(kr[j][d], qh[d], d0); d1 = dot2(kr[j][d], ql[d], d1); }
        float v = d0 + d1;
        asm volatile("" : "+v"(v));
        if constexpr (decltype(refill)::value) kr[j] = ldnt(kR, voff + j * 1024, soff);
        s[j] = row_sum(v);
      }
      float mt = m;
#pragma unroll
      for (int j = 0; j < GT; ++j) mt = fmaxf(mt, s[j]);
      const float alpha = __builtin_amdgcn_exp2f(m - mt);
      m = mt;
      float ps = 0.f;
#pragma unroll
      for (int j = 0; j < GT; ++j) { s[j] = __builtin_amdgcn_exp2f(s[j] - mt); ps += s[j]; }
      l = l * alpha + ps;
#pragma unroll
      for (int e = 0; e < 8; ++e) acc[e] *= alpha;
#pragma unroll
      for (int j = 0; j < GT; ++j) {
#pragma unroll
        for (int d = 0; d < 4; ++d) {
          acc[2 * d] = fmaf(s[j], bflo(vr[j][d]), acc[2 * d]);
          acc[2 * d + 1] = fmaf(s[j], bfhi(vr[j][d]), acc[2 * d + 1]);
        }
        asm volatile("" : "+v"(acc[0]), "+v"(acc[1]), "+v"(acc[2]), "+v"(acc[3]), "+v"(acc[4]), "+v"(acc[5]), "+v"(acc[6]), "+v"(acc[7]));
        if constexpr (decltype(refill)::value) vr[j] = ldnt(vR, voff + j * 1024, soff);
      }
    };
    for (int it = 1; it < my_tiles; ++it) {
      soff += NA * GT * 1024;
      tile(std::true_type{});
    }
    tile(std::false_type{});
    {
      float mw = fmaxf(m, xor16(m));
      mw = fmaxf(mw, xor32(mw));
      const float f = __builtin_amdgcn_exp2f(m - mw);
      l *= f;
      l += xor16(l); l += xor32(l);
#pragma unroll
      for (int e = 0; e < 8; ++e) { float a = acc[e] * f; a += xor16(a); a += xor32(a); acc[e] = a; }
      if (lane < 16) {
        *(f32x4*)&sm.opart[w][8 * lane] = f32x4{acc[0], acc[1], acc[2], acc[3]};
        *(f32x4*)&sm.opart[w][8 * lane + 4] = f32x4{acc[4], acc[5], acc[6], acc[7]};
      }
      if (lane == 0) { sm.ml[w][0] = mw; sm.ml[w][1] = l; }
    }
    wg_barrier();
    wg_barrier();
    wg_barrier();
    return;
  }

  const u16* xb = x + b * DM;
  u32x4 wQ[16], wG[18], wU[18], wD[18], wO[16];
  const rsrc_t rQ = mk_rsrc(wq, DM * NH * DH * 2), rG = mk_rsrc(wg, DM * DF * 2), rU = mk_rsrc(wu, DM * DF * 2);
  const rsrc_t rD = mk_rsrc(wd, DF * DM * 2), rO = mk_rsrc(wo, NH * DH * DM * 2);
  const int qc = t & 15, qrg = t >> 4;
  const int gc = t % 18, grg = t / 18;
  const int dc = t % 72, drg = t / 72;
  const unsigned qoff = (unsigned)(16 * qrg) * (NH * DH * 2) + 16 * qc;
  const unsigned goff = (unsigned)(18 * grg) * (DF * 2) + 16 * gc;
  const unsigned doff = (unsigned)(18 * drg) * (DM * 2) + 16 * dc;
  const unsigned ooff = (unsigned)(16 * drg) * (DM * 2) + 16 * dc;
#define LD_Q(i) wQ[i] = ldw(rQ, qoff, DH * 2 * h + (i) * (NH * DH * 2))
#define LD_G(i) wG[i] = ldw(rG, goff, 288 * h + (i) * (DF * 2))
#define LD_U(i) wU[i] = ldw(rU, goff, 288 * h + (i) * (DF * 2))
#define LD_D(i) wD[i] = ldw(rD, doff, (144 * h + (i)) * (DM * 2))
#define LD_O(i) wO[i] = ldw(rO, ooff, (DH * h + (i)) * (DM * 2))
#define SCHED_FENCE() __builtin_amdgcn_sched_barrier(0)
  const u32x4 xq0 = *(const GAS u32x4*)(xb + 16 * qrg), xq1 = *(const GAS u32x4*)(xb + 16 * qrg + 8);
  const u16 xme = xb[t], gwme = gw[t];
#pragma unroll
  for (int i = 0; i < 16; ++i) LD_Q(i);
  const unsigned ep_v = __hip_atomic_load((GAS unsigned*)(epoch + b * 64), __ATOMIC_RELAXED, __HIP_MEMORY_SCOPE_AGENT);
#pragma unroll
  for (int i = 0; i < 12; ++i) LD_G(i);
  SCHED_FENCE();
  if (t < 4) sm.ctr[t] = 0u;
  ((u16*)sm.xs)[t] = xme;
  {
    float gs = row_sum(bflo(xme) * bflo(gwme));
    gs += xor16(gs); gs += xor32(gs);
    if (lane == 0) sm.gpart[wave] = gs;
  }
  SCHED_FENCE();
  {
    float acc[8];
#pragma unroll
    for (int e = 0; e < 8; ++e) acc[e] = 0.f;
#pragma unroll
    for (int j = 0; j < 4; ++j) gemv_pair(acc, wQ[2 * j], wQ[2 * j + 1], xq0[j]);
#pragma unroll
    for (int j = 0; j < 4; ++j) gemv_pair(acc, wQ[8 + 2 * j], wQ[8 + 2 * j + 1], xq1[j]);
#pragma unroll
    for (int e = 0; e < 8; ++e) { float a = acc[e]; a += xor16(a); a += xor32(a); acc[e] = a; }
    if (lane < 16) {
      *(f32x4*)&sm.qpart[wave][8 * lane] = f32x4{acc[0], acc[1], acc[2], acc[3]};
      *(f32x4*)&sm.qpart[wave][8 * lane + 4] = f32x4{acc[4], acc[5], acc[6], acc[7]};
    }
  }
  wg_barrier();
  SCHED_FENCE();
#pragma unroll
  for (int i = 12; i < 18; ++i) LD_G(i);
#pragma unroll
  for (int i = 0; i < 10; ++i) LD_U(i);
  SCHED_FENCE();
  const unsigned tag = __builtin_amdgcn_readfirstlane(ep_v) + TAG_STEP;
  u64* myslots = slots + (size_t)(b * NH + h) * DM;
  __hip_atomic_store((GAS u64*)(myslots + t), (u64)(~tag) << 32, __ATOMIC_RELAXED, __HIP_MEMORY_SCOPE_AGENT);
  SCHED_FENCE();
  {
    float acc[8];
#pragma unroll
    for (int e = 0; e < 8; ++e) acc[e] = 0.f;
#pragma unroll
    for (int j = 0; j < 9; ++j) {
      gemv_pair(acc, wG[2 * j], wG[2 * j + 1], sm.xs[9 * grg + j]);
      SCHED_FENCE();
      if (j < 4) { LD_U(10 + 2 * j); LD_U(11 + 2 * j); } else { LD_D(2 * j - 8); LD_D(2 * j - 7); }
      SCHED_FENCE();
    }
    *(f32x4*)&sm.gu[0][grg][8 * gc] = f32x4{acc[0], acc[1], acc[2], acc[3]};
    *(f32x4*)&sm.gu[0][grg][8 * gc + 4] = f32x4{acc[4], acc[5], acc[6], acc[7]};
  }
  SCHED_FENCE();
  {
    float acc[8];
#pragma unroll
    for (int e = 0; e < 8; ++e) acc[e] = 0.f;
#pragma unroll
    for (int j = 0; j < 9; ++j) {
      gemv_pair(acc, wU[2 * j], wU[2 * j + 1], sm.xs[9 * grg + j]);
      SCHED_FENCE();
      if (j < 4) { LD_D(10 + 2 * j); LD_D(11 + 2 * j); } else { LD_O(2 * j - 8); LD_O(2 * j - 7); }
      SCHED_FENCE();
    }
    *(f32x4*)&sm.gu[1][grg][8 * gc] = f32x4{acc[0], acc[1], acc[2], acc[3]};
    *(f32x4*)&sm.gu[1][grg][8 * gc + 4] = f32x4{acc[4], acc[5], acc[6], acc[7]};
  }
  SCHED_FENCE();
  auto side_sync = [&](int k) {
    asm volatile("s_waitcnt lgkmcnt(0)" ::: "memory");
    if (lane == 0) __hip_atomic_fetch_add((LAS unsigned*)&sm.ctr[k], 1u, __ATOMIC_RELAXED, __HIP_MEMORY_SCOPE_WORKGROUP);
    while (__builtin_amdgcn_readfirstlane(__hip_atomic_load((LAS unsigned*)&sm.ctr[k], __ATOMIC_RELAXED, __HIP_MEMORY_SCOPE_WORKGROUP)) < (unsigned)NS)
      __builtin_amdgcn_s_sleep(2);
    asm volatile("" ::: "memory");
  };
  side_sync(0);
  if (t < 144) {
    float g = 0.f, u = 0.f;
#pragma unroll 8
    for (int r = 0; r < 32; ++r) { g += sm.gu[0][r][t]; u += sm.gu[1][r][t]; }
    const float inner = 0.7978845608028654f * (g + 0.044715f * g * g * g);
    const float e = __expf(-2.f * inner);
    const float gel = g / (1.f + e);
    sm.hs[t] = gel * u;
  }
  side_sync(1);
  float dacc[8];
#pragma unroll
  for (int e = 0; e < 8; ++e) dacc[e] = 0.f;
#pragma unroll
  for (int i = 0; i < 18; ++i) {
    const float hv = sm.hs[18 * drg + i];
#pragma unroll
    for (int d = 0; d < 4; ++d) {
      dacc[2 * d] = fmaf(hv, bflo(wD[i][d]), dacc[2 * d]);
      dacc[2 * d + 1] = fmaf(hv, bfhi(wD[i][d]), dacc[2 * d + 1]);
    }
  }
  SCHED_FENCE();
#pragma unroll
  for (int i = 10; i < 16; ++i) LD_O(i);
  SCHED_FENCE();
  float gate;
  {
    float z = 0.f;
#pragma unroll
    for (int s = 0; s < NS; ++s) z += sm.gpart[s];
    gate = 1.f / (1.f + __expf(-z));
  }
#pragma unroll
  for (int e = 0; e < 8; ++e) dacc[e] *= (1.f - gate);
  unsigned wp[8][8];
#pragma unroll
  for (int j = 0; j < 8; ++j)
#pragma unroll
    for (int d = 0; d < 4; ++d) {
      wp[j][2 * d] = pk_lo(wO[2 * j][d], wO[2 * j + 1][d]);
      wp[j][2 * d + 1] = pk_hi(wO[2 * j][d], wO[2 * j + 1][d]);
    }
#pragma unroll
  for (int j = 0; j < 8; ++j)
    asm volatile("" : "+v"(wp[j][0]), "+v"(wp[j][1]), "+v"(wp[j][2]), "+v"(wp[j][3]), "+v"(wp[j][4]), "+v"(wp[j][5]), "+v"(wp[j][6]), "+v"(wp[j][7]));
  asm volatile("" : "+v"(dacc[0]), "+v"(dacc[1]), "+v"(dacc[2]), "+v"(dacc[3]), "+v"(dacc[4]), "+v"(dacc[5]), "+v"(dacc[6]), "+v"(dacc[7]));
  __builtin_amdgcn_s_setprio(3);
  int t2 = t;
  asm volatile("" : "+v"(t2));
  const int dc2 = t2 % 72, drg2 = t2 / 72, gc2 = t2 % 18, grg2 = t2 / 18;
  wg_barrier();
  {
    float M = sm.ml[0][0];
#pragma unroll
    for (int w = 1; w < NA; ++w) M = fmaxf(M, sm.ml[w][0]);
    float f[NA], L = 0.f;
#pragma unroll
    for (int w = 0; w < NA; ++w) { f[w] = __builtin_amdgcn_exp2f(sm.ml[w][0] - M); L += f[w] * sm.ml[w][1]; }
    const float inv = 1.f / L;
#pragma unroll
    for (int w = 0; w < NA; ++w) f[w] *= inv;
    float acc[8];
#pragma unroll
    for (int e = 0; e < 8; ++e) acc[e] = 0.f;
#pragma unroll
    for (int q4 = 0; q4 < 4; ++q4) {
      f32x4 o = *(const f32x4*)&sm.opart[0][16 * drg2 + 4 * q4] * f[0];
#pragma unroll
      for (int w = 1; w < NA; ++w) o += *(const f32x4*)&sm.opart[w][16 * drg2 + 4 * q4] * f[w];
      const unsigned p0 = pack2(o[0], o[1]), p1 = pack2(o[2], o[3]);
#pragma unroll
      for (int e = 0; e < 8; ++e) { acc[e] = dot2(wp[2 * q4][e], p0, acc[e]); acc[e] = dot2(wp[2 * q4 + 1][e], p1, acc[e]); }
    }
#pragma unroll
    for (int e = 0; e < 8; ++e) acc[e] = fmaf(gate, acc[e], dacc[e]);
    *(f32x4*)&sm.fin[drg2][8 * dc2] = f32x4{acc[0], acc[1], acc[2], acc[3]};
    *(f32x4*)&sm.fin[drg2][8 * dc2 + 4] = f32x4{acc[4], acc[5], acc[6], acc[7]};
  }
  wg_barrier();
  {
    float s = 0.f;
#pragma unroll
    for (int r = 0; r < 8; ++r) s += sm.fin[r][t2];
    asm volatile("s_waitcnt vmcnt(0)" ::: "memory");
    __hip_atomic_store((GAS u64*)(myslots + t2), ((u64)tag << 32) | __builtin_bit_cast(unsigned, s), __ATOMIC_RELAXED, __HIP_MEMORY_SCOPE_AGENT);
  }
  {
    const GAS u64* src = (const GAS u64*)(slots + (size_t)(b * NH + grg2) * DM + 18 * h + gc2);
    u64 v = __hip_atomic_load(src, __ATOMIC_RELAXED, __HIP_MEMORY_SCOPE_AGENT);
    unsigned spins = 0;
    while ((unsigned)(v >> 32) != tag) {
      __builtin_amdgcn_s_sleep(4);
      v = __hip_atomic_load(src, __ATOMIC_RELAXED, __HIP_MEMORY_SCOPE_AGENT);
      if (++spins > SPIN_CAP) break;
    }
    sm.gath[grg2][gc2] = __builtin_bit_cast(float, (unsigned)v);
  }
  wg_barrier();
  if (t < 18) {
    float s = 0.f;
#pragma unroll
    for (int p = 0; p < 32; ++p) s += sm.gath[p][t];
    out[b * DM + 18 * h + t] = (u16)f2bf(s);
  }
  if (h == 0 && t == 64) __hip_atomic_store((GAS unsigned*)(epoch + b * 64), tag, __ATOMIC_RELAXED, __HIP_MEMORY_SCOPE_AGENT);
}

extern "C" void kernel_launch(void* const* d_in, const int* in_sizes, int n_in,
                              void* d_out, int out_size, void* d_ws, size_t ws_size,
                              hipStream_t stream) {
  (void)in_sizes; (void)n_in; (void)out_size; (void)ws_size;
  unsigned* epoch = (unsigned*)d_ws;
  u64* slots = (u64*)((char*)d_ws + 4096);
  fused_decode_kernel<<<dim3(256), dim3(NTHREADS), 0, stream>>>(
      (const u16*)d_in[0], (const u32x4*)d_in[1], (const u32x4*)d_in[2], (const u16*)d_in[3], (const u16*)d_in[4],
      (const u16*)d_in[6], (const u16*)d_in[7], (const u16*)d_in[8], (const u16*)d_in[9],
      (u16*)d_out, slots, epoch);
}
```

```cpp
#include <hip/hip_runtime.h>
#include <hip/hip_bf16.h>

#define GAS __attribute__((address_space(1)))
#define LAS __attribute__((address_space(3)))
typedef unsigned u32x4 __attribute__((ext_vector_type(4)));
typedef unsigned u32x2 __attribute__((ext_vector_type(2)));
typedef float f32x4 __attribute__((ext_vector_type(4)));
typedef float f32x2 __attribute__((ext_vector_type(2)));
typedef __bf16 bf16x2 __attribute__((ext_vector_type(2)));
typedef __bf16 bf16x8 __attribute__((ext_vector_type(8)));
typedef unsigned short u16;
typedef unsigned long long u64;

#define WIN 7
#define SWIN 2
#define OWIN SWIN
#define HSLP 64
#define MSLP 127
#define ASLP 4
#define XQ 1
#define XO 1
#define XW 1
#define NA 3
#define NS 9
#define NTHREADS ((NS + NA) * 64)

constexpr int DM = 576, NH = 32, SK = 4096, DH = 128, DF = 4608;
constexpr int NGU = 144, NDN = 108;
constexpr u64 TAG_STEP = 0x9E3779B97Full;
constexpr unsigned SPIN_CAP = 1u << 18;

__device__ __forceinline__ float dot2(unsigned a, unsigned b, float c) {
  return __builtin_amdgcn_fdot2_f32_bf16(__builtin_bit_cast(bf16x2, a), __builtin_bit_cast(bf16x2, b), c, false);
}
__device__ __forceinline__ float bflo(unsigned v) { return __builtin_bit_cast(float, v << 16); }
__device__ __forceinline__ float bfhi(unsigned v) { return __builtin_bit_cast(float, v & 0xffff0000u); }
__device__ __forceinline__ unsigned pk_lo(unsigned a, unsigned b) { return __builtin_amdgcn_perm(b, a, 0x05040100u); }
__device__ __forceinline__ unsigned pk_hi(unsigned a, unsigned b) { return __builtin_amdgcn_perm(b, a, 0x07060302u); }
__device__ __forceinline__ unsigned pack2(float a, float b) {
  return __builtin_bit_cast(unsigned, __builtin_convertvector(f32x2{a, b}, bf16x2));
}
__device__ __forceinline__ unsigned f2bf(float f) { return pack2(f, 0.f) & 0xffffu; }
template <int CTRL> __device__ __forceinline__ float dpp(float v) {
  return __builtin_bit_cast(float, __builtin_amdgcn_update_dpp(0, __builtin_bit_cast(int, v), CTRL, 0xf, 0xf, true));
}
__device__ __forceinline__ float row_sum(float v) {
  v += dpp<0x128>(v); v += dpp<0x124>(v); v += dpp<0x122>(v); v += dpp<0x121>(v);
  return v;
}
__device__ __forceinline__ float row_max(float v) {
  v = fmaxf(v, dpp<0x128>(v)); v = fmaxf(v, dpp<0x124>(v)); v = fmaxf(v, dpp<0x122>(v)); v = fmaxf(v, dpp<0x121>(v));
  return v;
}
__device__ __forceinline__ float asf(unsigned u) { return __builtin_bit_cast(float, u); }
__device__ __forceinline__ unsigned asu(float f) { return __builtin_bit_cast(unsigned, f); }
__device__ __forceinline__ float sum32(float a, float b) {
  const auto r = __builtin_amdgcn_permlane32_swap(asu(a), asu(b), false, false);
  return asf(r[0]) + asf(r[1]);
}
__device__ __forceinline__ float sum16(float a, float b) {
  const auto r = __builtin_amdgcn_permlane16_swap(asu(a), asu(b), false, false);
  return asf(r[0]) + asf(r[1]);
}
__device__ __forceinline__ float wave_sum(float v) { v = row_sum(v); v = sum16(v, v); return sum32(v, v); }
__device__ __forceinline__ float wave_max(float v) {
  v = row_max(v);
  const auto r = __builtin_amdgcn_permlane16_swap(asu(v), asu(v), false, false);
  v = fmaxf(asf(r[0]), asf(r[1]));
  const auto s = __builtin_amdgcn_permlane32_swap(asu(v), asu(v), false, false);
  return fmaxf(asf(s[0]), asf(s[1]));
}
__device__ __forceinline__ f32x4 mfma(u32x4 a, u32x4 b, f32x4 c) {
  return __builtin_amdgcn_mfma_f32_16x16x32_bf16(__builtin_bit_cast(bf16x8, a), __builtin_bit_cast(bf16x8, b), c, 0, 0, 0);
}
__device__ __forceinline__ void wg_barrier() {
  asm volatile("s_waitcnt lgkmcnt(0)" ::: "memory");
  __builtin_amdgcn_s_barrier();
  asm volatile("" ::: "memory");
}
typedef __amdgpu_buffer_rsrc_t rsrc_t;
__device__ __forceinline__ rsrc_t mk_rsrc(const void* p, unsigned bytes) { return __builtin_amdgcn_make_buffer_rsrc((void*)p, (short)0, (int)bytes, 0x00020000); }
__device__ __forceinline__ u32x4 ld16(rsrc_t r, unsigned voff, unsigned soff) { return __builtin_amdgcn_raw_buffer_load_b128(r, (int)voff, (int)soff, 2); }
__device__ __forceinline__ u32x2 ld8(rsrc_t r, unsigned voff, unsigned soff) { return __builtin_amdgcn_raw_buffer_load_b64(r, (int)voff, (int)soff, 2); }
#define SCHED_FENCE() __builtin_amdgcn_sched_barrier(0)
#define VMWAIT(n) asm volatile("s_waitcnt vmcnt(%0)" :: "n"(n))

#define STAMP(k)
#define STAMP_INIT()

__device__ __forceinline__ u64 mk_gran(u64 tag, float v) {
  const unsigned u = __builtin_bit_cast(unsigned, v);
  const unsigned r = ((u & 0x7f800000u) == 0x7f800000u) ? (u | ((u & 0xffu) ? 0x100u : 0u)) : u + 0x80u;
  return (tag << 24) | (u64)(r >> 8);
}
__device__ __forceinline__ float gran_val(u64 g) { return __builtin_bit_cast(float, (unsigned)g << 8); }
__device__ __forceinline__ void put_gran(u64* p, u64 g) { __hip_atomic_store((GAS u64*)p, g, __ATOMIC_RELAXED, __HIP_MEMORY_SCOPE_AGENT); }
__device__ __forceinline__ u64 get_gran(const u64* p) { return __hip_atomic_load((const GAS u64*)p, __ATOMIC_RELAXED, __HIP_MEMORY_SCOPE_AGENT); }
#define EXP 0
#define LAUX 18
#define SAUX 18
template <int SLEEP> __device__ __forceinline__ float wait_gran(const u64* p, u64 tag) {
  u64 v = get_gran(p);
  unsigned spins = 0;
#pragma nounroll
  while ((v >> 24) != tag) {
    __builtin_amdgcn_s_sleep(SLEEP);
    v = get_gran(p);
    if (++spins > SPIN_CAP) break;
  }
  return gran_val(v);
}
__device__ __forceinline__ void put_gran2(rsrc_t r, unsigned idx, u64 g) {
  __builtin_amdgcn_raw_buffer_store_b64(u32x2{(unsigned)g, (unsigned)(g >> 32)}, r, (int)(idx * 8), 0, SAUX);
}
__device__ __forceinline__ u64 get_gran2(rsrc_t r, unsigned idx) {
  const u32x2 v = __builtin_amdgcn_raw_buffer_load_b64(r, (int)(idx * 8), 0, LAUX);
  asm volatile("" ::: "memory");
  return ((u64)v[1] << 32) | v[0];
}
template <int SLEEP> __device__ __forceinline__ float wait_gran2(rsrc_t r, unsigned idx, u64 tag) {
  if (EXP & 1) return 0.5f;
  u64 v = get_gran2(r, idx);
  if (EXP & 2) return gran_val(v);
  unsigned spins = 0;
#pragma nounroll
  while ((v >> 24) != tag) {
    __builtin_amdgcn_s_sleep(SLEEP);
    v = get_gran2(r, idx);
    if (++spins > SPIN_CAP) break;
  }
  return gran_val(v);
}

struct __attribute__((aligned(16))) Smem {
  float qpart[NS][DH];
  float img[NA][22][64];
  float red[6 * 16 * 64];
  unsigned hA[16][68];
  unsigned oA[12][68];
  float gath[18][33];
  float mg[36 * 18];
  float msum[32];
  float ml[4][2];
  float gpart[16];
  unsigned ctr[8];
};

template <int L> __device__ __forceinline__ float merge(float a, float b, int lane) {
  constexpr int CTRL[4] = {0x128, 0x141, 0x4E, 0xB1};
  const bool sel = (lane >> (3 - L)) & 1;
  const float x = sel ? b : a, y = sel ? a : b;
  return x + dpp<CTRL[L]>(y);
}
constexpr int bitrev4(int i) { return ((i & 1) << 3) | ((i & 2) << 1) | ((i & 4) >> 1) | ((i & 8) >> 3); }

struct QFrag { unsigned h[4], l[4]; };
__device__ __forceinline__ QFrag build_q(const Smem& sm, int lane) {
  QFrag q;
  const int c = lane & 15;
  f32x4 a = {0.f, 0.f, 0.f, 0.f}, bb = {0.f, 0.f, 0.f, 0.f};
#pragma unroll
  for (int s = 0; s < NS; ++s) {
    a += *(const f32x4*)&sm.qpart[s][8 * c];
    bb += *(const f32x4*)&sm.qpart[s][8 * c + 4];
    if (s % 3 == 2) { asm volatile("" : "+v"(a), "+v"(bb)); SCHED_FENCE(); }
  }
  const float sc = 0.08838834764831845f * 1.4426950408889634f;
  const float v[8] = {a[0] * sc, a[1] * sc, a[2] * sc, a[3] * sc, bb[0] * sc, bb[1] * sc, bb[2] * sc, bb[3] * sc};
#pragma unroll
  for (int d = 0; d < 4; ++d) {
    const unsigned hi = pack2(v[2 * d], v[2 * d + 1]);
    q.h[d] = hi;
    q.l[d] = pack2(v[2 * d] - bflo(hi), v[2 * d + 1] - bfhi(hi));
  }
  return q;
}
__device__ __forceinline__ float qk(const u32x4& k, const QFrag& q) {
  float d0 = dot2(k[0], q.h[0], 0.f), d1 = dot2(k[0], q.l[0], 0.f);
#pragma unroll
  for (int d = 1; d < 4; ++d) { d0 = dot2(k[d], q.h[d], d0); d1 = dot2(k[d], q.l[d], d1); }
  return d0 + d1;
}
template <int J> __device__ __forceinline__ u32x4 bfrag(const u32x2* r) {
  u32x4 f;
#pragma unroll
  for (int d = 0; d < 4; ++d) f[d] = (J & 1) ? pk_hi(r[2 * d][J >> 1], r[2 * d + 1][J >> 1]) : pk_lo(r[2 * d][J >> 1], r[2 * d + 1][J >> 1]);
  return f;
}

__global__ void __launch_bounds__(NTHREADS)
fused_decode_kernel(const u16* __restrict__ x, const u32x4* __restrict__ Kc, const u32x4* __restrict__ Vc,
                    const u16* __restrict__ wq, const u16* __restrict__ wo, const u16* __restrict__ wg,
                    const u16* __restrict__ wu, const u16* __restrict__ wd, const u16* __restrict__ gw,
                    u16* __restrict__ out, u64* slotA, u64* slotM, u64* slotG, u64* alive, u64* epoch) {
  __shared__ Smem sm;
  const int t = threadIdx.x, lane = t & 63;
  const int wave = __builtin_amdgcn_readfirstlane(t >> 6);
  const int bid = blockIdx.x;
  const int h = bid & 31;
  const int b = bid >> 5;
  const size_t base = (size_t)bid * (SK * DH / 8);
  const unsigned voff = lane * 16;
  STAMP_INIT();

  if (wave >= NS) {
    __builtin_amdgcn_s_setprio(2);
    const int w = wave - NS;
    const rsrc_t vR = mk_rsrc(Vc + base, SK * DH * 2);
    unsigned nso = w * 8192;
    u32x4 r[4][8];
    {
      const rsrc_t kR = mk_rsrc(Kc + base, SK * DH * 2);
#pragma unroll
      for (int st = 0; st < 4; ++st) {
#pragma unroll
        for (int j = 0; j < 8; ++j) { VMWAIT(WIN - 1); r[st][j] = ld16(kR, voff, nso + j * 1024); SCHED_FENCE(); }
        nso += NA * 8192;
      }
    }
    wg_barrier();
    STAMP(1);
    float mx = -INFINITY;
    {
      const QFrag q = build_q(sm, lane);
#pragma nounroll
      for (int it = 0; it < 10; ++it) {
        const rsrc_t nR = mk_rsrc((it < 9 ? Kc : Vc) + base, SK * DH * 2);
        if (it == 9) nso = w * 8192;
#pragma unroll
        for (int pr = 0; pr < 2; ++pr) {
          float lv[4], v;
#pragma unroll
          for (int idx = 0; idx < 16; ++idx) {
            u32x4& k = r[2 * pr + (idx >> 3)][idx & 7];
            v = qk(k, q);
            asm volatile("" : "+v"(v));
            VMWAIT(WIN - 1); k = ld16(nR, voff, nso + (idx & 7) * 1024);
            SCHED_FENCE();
            if ((idx & 7) == 7) nso += NA * 8192;
            if (!(idx & 1)) { lv[0] = v; continue; }
            v = merge<0>(lv[0], v, lane);
            if (!(idx & 2)) { lv[1] = v; continue; }
            v = merge<1>(lv[1], v, lane);
            if (!(idx & 4)) { lv[2] = v; continue; }
            v = merge<2>(lv[2], v, lane);
            if (!(idx & 8)) { lv[3] = v; continue; }
            v = merge<3>(lv[3], v, lane);
          }
          sm.img[w][2 * it + pr][lane] = v;
          mx = fmaxf(mx, v);
        }
      }
    }
    while (__builtin_amdgcn_readfirstlane(__hip_atomic_load((LAS unsigned*)&sm.ctr[0], __ATOMIC_RELAXED, __HIP_MEMORY_SCOPE_WORKGROUP)) < 8u)
      __builtin_amdgcn_s_sleep(1);
    asm volatile("" ::: "memory");
    float e0 = sm.img[w][20][lane], e1 = sm.img[w][21][lane];
    if ((lane & 1) || w == 2) e1 = -INFINITY;
    mx = wave_max(fmaxf(mx, fmaxf(e0, e1)));
    e0 = __builtin_amdgcn_exp2f(e0 - mx);
    e1 = __builtin_amdgcn_exp2f(e1 - mx);
    float l = e0 + e1;
    f32x2 acc[4];
#pragma unroll
    for (int d = 0; d < 4; ++d) acc[d] = f32x2{0.f, 0.f};
    auto vtile = [&](u32x4 (&vt)[8], float p, auto halfc, auto modec) {
      constexpr int HALF = decltype(halfc)::value, MODE = decltype(modec)::value;
      auto piece = [&](auto jc) {
        constexpr int j = decltype(jc)::value;
        const float pb = dpp<0x150 + bitrev4(8 * HALF + j)>(p);
#pragma unroll
        for (int d = 0; d < 4; ++d) acc[d] = __builtin_elementwise_fma(f32x2{bflo(vt[j][d]), bfhi(vt[j][d])}, f32x2{pb, pb}, acc[d]);
        asm volatile("" : "+v"(acc[0]), "+v"(acc[1]), "+v"(acc[2]), "+v"(acc[3]));
        if constexpr (MODE == 1) VMWAIT(WIN - 1);
        if constexpr (MODE == 2) asm volatile("s_cmp_ge_u32 %0, %1\n\ts_cbranch_scc1 1f\n\ts_waitcnt vmcnt(%2)\n1:" :: "s"(nso), "n"(SK * DH * 2), "n"(WIN - 1) : "scc");
        if constexpr (MODE != 0) vt[j] = ld16(vR, voff, nso + j * 1024);
        SCHED_FENCE();
      };
      piece(std::integral_constant<int, 0>{}); piece(std::integral_constant<int, 1>{}); piece(std::integral_constant<int, 2>{}); piece(std::integral_constant<int, 3>{});
      piece(std::integral_constant<int, 4>{}); piece(std::integral_constant<int, 5>{}); piece(std::integral_constant<int, 6>{}); piece(std::integral_constant<int, 7>{});
      if constexpr (MODE != 0) nso += NA * 8192;
    };
    using I0 = std::integral_constant<int, 0>; using I1 = std::integral_constant<int, 1>; using I2 = std::integral_constant<int, 2>;
    float s0 = sm.img[w][0][lane], s1 = sm.img[w][1][lane];
#pragma nounroll
    for (int it = 0; it < 9; ++it) {
      const float p0 = __builtin_amdgcn_exp2f(s0 - mx), p1 = __builtin_amdgcn_exp2f(s1 - mx);
      l += p0 + p1;
      s0 = sm.img[w][2 * it + 2][lane]; s1 = sm.img[w][2 * it + 3][lane];
      vtile(r[0], p0, I0{}, I1{}); vtile(r[1], p0, I1{}, I1{});
      vtile(r[2], p1, I0{}, I1{}); vtile(r[3], p1, I1{}, I1{});
    }
    {
      const float p0 = __builtin_amdgcn_exp2f(s0 - mx), p1 = __builtin_amdgcn_exp2f(s1 - mx);
      l = wave_sum(l + p0 + p1);
      if (lane == 0) { sm.ml[w][0] = mx; sm.ml[w][1] = l; }
      vtile(r[0], p0, I0{}, I1{}); vtile(r[1], p0, I1{}, I1{});
      vtile(r[2], p1, I0{}, I2{}); vtile(r[3], p1, I1{}, I0{});
      vtile(r[0], e0, I0{}, I0{}); vtile(r[1], e0, I1{}, I0{});
      if (w < 2) vtile(r[2], e1, I0{}, I0{});
    }
    STAMP(3);
    *(u32x4*)&sm.oA[4 * w + (lane >> 4)][4 * (lane & 15)] =
        u32x4{pack2(acc[0][0], acc[0][1]), pack2(acc[1][0], acc[1][1]), pack2(acc[2][0], acc[2][1]), pack2(acc[3][0], acc[3][1])};
    wg_barrier();
    wg_barrier();
    return;
  }

  const int n = lane & 15, kb = lane >> 4;
  const u16* xb = x + b * DM;
  u64 tag;
  u32x4 kx[8];
  u32x4 xg0, xg1;
  {
    u32x4 wQ[16];
    const u64 ep_v = get_gran(epoch);
    const u32x4 xq0 = *(const GAS u32x4*)(xb + 64 * wave + 8 * kb), xq1 = *(const GAS u32x4*)(xb + 64 * wave + 32 + 8 * kb);
    const u16 xme = xb[t], gwme = gw[t];
    SCHED_FENCE();
    const rsrc_t rQ = mk_rsrc(wq, XQ * DM * NH * DH * 2);
    const rsrc_t kR = mk_rsrc(Kc + base, SK * DH * 2);
    const unsigned qoff = (unsigned)(8 * kb) * (NH * DH * 2) + 16 * n;
#pragma unroll
    for (int i = 0; i < 16; ++i) wQ[i] = ld16(rQ, qoff, (64 * wave + 32 * (i >> 3) + (i & 7)) * (NH * DH * 2) + DH * 2 * h);
    SCHED_FENCE();
    if (t < 8) sm.ctr[t] = 0u;
    {
      const float gsum = wave_sum(bflo(xme) * bflo(gwme));
      if (lane == 0) sm.gpart[wave] = gsum;
      const unsigned lo = __builtin_amdgcn_readfirstlane((unsigned)ep_v), hi = __builtin_amdgcn_readfirstlane((unsigned)(ep_v >> 32));
      tag = (((((u64)hi << 32) | lo) >> 24) + TAG_STEP) & 0xffffffffffull;
    }
    SCHED_FENCE();
    f32x4 d[8];
#pragma unroll
    for (int blk = 0; blk < 2; ++blk) {
#pragma unroll
      for (int j = 4 * blk; j < 4 * blk + 4; ++j) { VMWAIT(14 - j); kx[j] = ld16(kR, voff, (120 + wave) * 8192 + j * 1024); SCHED_FENCE(); }
#pragma unroll
      for (int j = 0; j < 8; ++j) {
        u32x4 f;
#pragma unroll
        for (int e = 0; e < 4; ++e) {
          const unsigned lo = wQ[8 * blk + 2 * e][j >> 1], hi = wQ[8 * blk + 2 * e + 1][j >> 1];
          f[e] = (j & 1) ? pk_hi(lo, hi) : pk_lo(lo, hi);
        }
        d[j] = blk ? mfma(xq1, f, d[j]) : mfma(xq0, f, f32x4{0.f, 0.f, 0.f, 0.f});
      }
      SCHED_FENCE();
    }
    if (lane < 16) {
      *(f32x4*)&sm.qpart[wave][8 * lane] = f32x4{d[0][0], d[1][0], d[2][0], d[3][0]};
      *(f32x4*)&sm.qpart[wave][8 * lane + 4] = f32x4{d[4][0], d[5][0], d[6][0], d[7][0]};
    }
  }
  wg_barrier();
  SCHED_FENCE();
  xg0 = *(const GAS u32x4*)(x + (n & 7) * DM + 64 * wave + 8 * kb); xg1 = *(const GAS u32x4*)(x + (n & 7) * DM + 64 * wave + 32 + 8 * kb);
  auto side_arrive = [&](int k) {
    asm volatile("s_waitcnt lgkmcnt(0)" ::: "memory");
    if (lane == 0) __hip_atomic_fetch_add((LAS unsigned*)&sm.ctr[k], 1u, __ATOMIC_RELAXED, __HIP_MEMORY_SCOPE_WORKGROUP);
  };
  auto side_sync = [&](int k) {
    side_arrive(k);
    while (__builtin_amdgcn_readfirstlane(__hip_atomic_load((LAS unsigned*)&sm.ctr[k], __ATOMIC_RELAXED, __HIP_MEMORY_SCOPE_WORKGROUP)) < (unsigned)NS)
      __builtin_amdgcn_s_sleep(2);
    asm volatile("" ::: "memory");
  };
  if (t == 0) put_gran(alive + bid, tag << 24);
  if (wave < 8) {
    const QFrag q = build_q(sm, lane);
    float v[8];
#pragma unroll
    for (int j = 0; j < 8; ++j) v[j] = qk(kx[j], q);
#pragma unroll
    for (int j = 0; j < 4; ++j) v[j] = merge<0>(v[2 * j], v[2 * j + 1], lane);
#pragma unroll
    for (int j = 0; j < 2; ++j) v[j] = merge<1>(v[2 * j], v[2 * j + 1], lane);
    float s = merge<2>(v[0], v[1], lane);
    s += dpp<0xB1>(s);
    const int e = wave / 3;
    if ((lane & 1) == (e & 1)) sm.img[wave % 3][20 + (e >> 1)][lane] = s;
    side_arrive(0);
  }
  SCHED_FENCE();
  u32x4 wo_f[4][4];
  {
    const rsrc_t rO = mk_rsrc(wo, XO * NH * DH * DM * 2);
    const unsigned ooff = (unsigned)(8 * kb) * (DM * 2) + 8 * n;
    u32x2 wr[32];
#pragma unroll
    for (int i = 0; i < 32; ++i) { VMWAIT(OWIN - 1); wr[i] = ld8(rO, ooff, (DH * h + 32 * (i >> 3) + (i & 7)) * (DM * 2) + 128 * wave); SCHED_FENCE(); }
#pragma unroll
    for (int blk = 0; blk < 4; ++blk) {
      wo_f[blk][0] = bfrag<0>(wr + 8 * blk); wo_f[blk][1] = bfrag<1>(wr + 8 * blk);
      wo_f[blk][2] = bfrag<2>(wr + 8 * blk); wo_f[blk][3] = bfrag<3>(wr + 8 * blk);
      asm volatile("" : "+v"(wo_f[blk][0]), "+v"(wo_f[blk][1]), "+v"(wo_f[blk][2]), "+v"(wo_f[blk][3]));
    }
  }
  SCHED_FENCE();
  constexpr unsigned OFF_G = 36 * 8 * DM;
  const rsrc_t rS = mk_rsrc(slotM, (OFF_G + NGU * 512) * 8);
  if (bid < NGU + NDN) {
    const bool isG = bid < NGU;
    const int dn = bid - NGU, kg = dn / 3, cg = dn % 3;
    const bool act = isG || wave < 6;
    u32x2 wr[16];
    if (act) {
      const rsrc_t rW = mk_rsrc(isG ? ((bid & 1) ? wu : wg) : wd, XW * DM * DF * 2);
      const unsigned rs = isG ? DF * 2 : DM * 2;
      const unsigned s0 = isG ? 64 * wave * (DF * 2) + 128 * (bid >> 1) : (128 * kg + 64 * (wave / 3)) * (DM * 2) + 384 * cg + 128 * (wave % 3);
      const unsigned woff = 8 * kb * rs + 8 * n;
#pragma unroll
      for (int i = 0; i < 16; ++i) { VMWAIT(SWIN - 1); wr[i] = ld8(rW, woff, s0 + (32 * (i >> 3) + (i & 7)) * rs); SCHED_FENCE(); }
    }
    if (!isG) {
      if (t < 512) {
#pragma unroll
        for (int i = 0; i < 2; ++i) {
          const unsigned pg = OFF_G + (2 * (2 * kg + i)) * 512 + t;
          const float g = wait_gran2<HSLP>(rS, pg, tag), up = wait_gran2<HSLP>(rS, pg + 512, tag);
          const float inner = 0.7978845608028654f * (g + 0.044715f * g * g * g);
          const float e = __expf(-2.f * inner);
          const float hv = g / (1.f + e) * up;
          const unsigned hi = f2bf(hv);
          u16* hp = (u16*)&sm.hA[t >> 6][0] + 64 * i + (t & 63);
          hp[0] = (u16)hi;
          hp[8 * 68 * 2] = (u16)f2bf(hv - bflo(hi));
        }
      }
      side_sync(1);
      const int k0 = 32 * ((wave / 3) & 1);
      xg0 = *(const u32x4*)&sm.hA[n][k0 + 4 * kb]; xg1 = *(const u32x4*)&sm.hA[n][k0 + 16 + 4 * kb];
    }
    if (act) {
      const f32x4 z = {0.f, 0.f, 0.f, 0.f};
      const f32x4 d0 = mfma(xg1, bfrag<0>(wr + 8), mfma(xg0, bfrag<0>(wr), z)), d1 = mfma(xg1, bfrag<1>(wr + 8), mfma(xg0, bfrag<1>(wr), z));
      const f32x4 d2 = mfma(xg1, bfrag<2>(wr + 8), mfma(xg0, bfrag<2>(wr), z)), d3 = mfma(xg1, bfrag<3>(wr + 8), mfma(xg0, bfrag<3>(wr), z));
      if (!isG || lane < 32) {
        float* rp = &sm.red[(wave * (isG ? 8 : 16) + 4 * kb) * 64 + 4 * n];
#pragma unroll
        for (int e = 0; e < 4; ++e) *(f32x4*)(rp + 64 * e) = f32x4{d0[e], d1[e], d2[e], d3[e]};
      }
    }
    side_sync(2);
    if (t < 512) {
      if (isG) {
        float s = 0.f;
#pragma unroll
        for (int r = 0; r < NS; ++r) s += sm.red[r * 512 + t];
        if (!(EXP & 4) || s == 123.f) put_gran2(rS, OFF_G + bid * 512 + t, mk_gran(tag, s));
      } else {
#pragma unroll
        for (int ln = 0; ln < 3; ++ln) {
          const float* rp = &sm.red[ln * 1024 + t];
          const float s = (rp[0] + rp[3072]) + (rp[512] + rp[3072 + 512]);
          if (!(EXP & 4) || s == 123.f) put_gran2(rS, (kg * 8 + (t >> 6)) * DM + 192 * cg + 64 * ln + (t & 63), mk_gran(tag, s));
        }
      }
    }
  }
  float gate;
  {
    float z = 0.f;
#pragma unroll
    for (int s = 0; s < NS; ++s) z += sm.gpart[s];
    gate = 1.f / (1.f + __expf(-z));
  }
  const int pp = t / 18, pc = t % 18;
  sm.mg[t] = wait_gran2<MSLP>(rS, (pp * 8 + b) * DM + 18 * h + pc, tag);
  if (t < 72) sm.mg[576 + t] = wait_gran2<MSLP>(rS, ((32 + pp) * 8 + b) * DM + 18 * h + pc, tag);
  if (bid == 0 && t < 256) (void)wait_gran<MSLP>(alive + t, tag);
  side_sync(3);
  if (t < 18) {
    float s = 0.f;
#pragma unroll
    for (int p = 0; p < 36; ++p) s += sm.mg[18 * p + t];
    sm.msum[t] = s * (1.f - gate);
  }
  STAMP(2);
  __builtin_amdgcn_s_setprio(3);
  wg_barrier();
  {
    const unsigned* ap = &sm.oA[n < 12 ? n : n - 12][4 * kb];
    const u32x4 a0 = *(const u32x4*)ap, a1 = *(const u32x4*)(ap + 16), a2 = *(const u32x4*)(ap + 32), a3 = *(const u32x4*)(ap + 48);
    const float m0 = sm.ml[0][0], m1 = sm.ml[1][0], m2 = sm.ml[2][0];
    const float M = fmaxf(m0, fmaxf(m1, m2));
    const float f0 = __builtin_amdgcn_exp2f(m0 - M), f1 = __builtin_amdgcn_exp2f(m1 - M), f2 = __builtin_amdgcn_exp2f(m2 - M);
    const float L = f0 * sm.ml[0][1] + f1 * sm.ml[1][1] + f2 * sm.ml[2][1];
    const float f = (kb == 0 ? f0 : kb == 1 ? f1 : kb == 2 ? f2 : 0.f) / L;
    float s[4];
#pragma unroll
    for (int j = 0; j < 4; ++j) {
      f32x4 d = {0.f, 0.f, 0.f, 0.f};
      d = mfma(a0, wo_f[0][j], d); d = mfma(a1, wo_f[1][j], d); d = mfma(a2, wo_f[2][j], d); d = mfma(a3, wo_f[3][j], d);
      s[j] = ((d[0] + d[1]) + (d[2] + d[3])) * f;
    }
    const float v = sum16(sum32(s[0], s[2]), sum32(s[1], s[3]));
    put_gran(slotA + (size_t)bid * DM + 64 * wave + 4 * n + kb, mk_gran(tag, v));
  }
  STAMP(4);
  sm.gath[pc][pp] = wait_gran<ASLP>(slotA + (size_t)(b * NH + pp) * DM + 18 * h + pc, tag);
  STAMP(5);
  wg_barrier();
  {
    const int col = 2 * wave + (lane >> 5);
    float s = row_sum(sm.gath[col][lane & 31]);
    s = sum16(s, s);
    if ((lane & 31) == 0) out[b * DM + 18 * h + col] = (u16)f2bf(fmaf(gate, s, sm.msum[col]));
  }
  STAMP(6);
  if (bid == 0 && t == 64) put_gran(epoch, tag << 24);
}

extern "C" void kernel_launch(void* const* d_in, const int* in_sizes, int n_in,
                              void* d_out, int out_size, void* d_ws, size_t ws_size,
                              hipStream_t stream) {
  (void)in_sizes; (void)n_in; (void)out_size; (void)ws_size;
  u64* epoch = (u64*)d_ws;
  u64* alive = (u64*)((char*)d_ws + 4096);
  u64* slotA = (u64*)((char*)d_ws + 8192);
#define MOFF 0
  u64* slotM = slotA + 256 * DM + (size_t)MOFF * 131072;
  u64* slotG = slotM + 36 * 8 * DM;
  fused_decode_kernel<<<dim3(256), dim3(NTHREADS), 0, stream>>>(
      (const u16*)d_in[0], (const u32x4*)d_in[1], (const u32x4*)d_in[2], (const u16*)d_in[3], (const u16*)d_in[4],
      (const u16*)d_in[6], (const u16*)d_in[7], (const u16*)d_in[8], (const u16*)d_in[9],
      (u16*)d_out, slotA, slotM, slotG, alive, epoch);
}
```

```cpp
#define DRY 0
#define HEV 0
#define HSTAT 1
#include <hip/hip_runtime.h>
#include <hip/hip_bf16.h>

#define GAS __attribute__((address_space(1)))
#define LAS __attribute__((address_space(3)))
typedef unsigned u32x4 __attribute__((ext_vector_type(4)));
typedef unsigned u32x2 __attribute__((ext_vector_type(2)));
typedef float f32x4 __attribute__((ext_vector_type(4)));
typedef float f32x2 __attribute__((ext_vector_type(2)));
typedef __bf16 bf16x2 __attribute__((ext_vector_type(2)));
typedef __bf16 bf16x8 __attribute__((ext_vector_type(8)));
typedef unsigned short u16;
typedef unsigned long long u64;

#define WIN 7
#define OWIN 3
#define GWIN 3
#define DWIN 3
#define TRIG 7
#define ASLP 4
#define CONT 0
#define NOXK 0
#define T19 0
#define SL 83.5
#define PACE() do { if (T19) { while ((int)(((unsigned)__builtin_amdgcn_s_memrealtime() << 8) - tgt) < 0) __builtin_amdgcn_s_sleep(1); tgt += (unsigned)(SL * 256); } } while (0)
#define WINK WIN
#define WINP WIN
#define WLO 6
#define WHI 9
#define LS 1.0
#define DB 30
__device__ constexpr int LINE[20] = {790, 1250, 1610, 2000, 2440, 2800, 3140, 3470, 3830, 4210, 4420, 4770, 5100, 5420, 5750, 6080, 6410, 6740, 7080, 7400};
#define W0 6
#define W1 7
#define W2 9
#define W3 9
#define W4 9
#define L0 -30
#define L1 30
#define L2 90
#define L3 170
#define MWAIT() asm volatile("s_waitcnt vmcnt(%1)\n\ts_cmp_gt_u32 %0, 3\n\ts_cbranch_scc1 1f\n\ts_waitcnt vmcnt(%2)\n\ts_cmp_gt_u32 %0, 2\n\ts_cbranch_scc1 1f\n\t" \
  "s_waitcnt vmcnt(%3)\n\ts_cmp_gt_u32 %0, 1\n\ts_cbranch_scc1 1f\n\ts_waitcnt vmcnt(%4)\n\ts_cmp_gt_u32 %0, 0\n\ts_cbranch_scc1 1f\n\ts_waitcnt vmcnt(%5)\n1:" \
  :: "s"(wm), "n"(W4 - 1), "n"(W3 - 1), "n"(W2 - 1), "n"(W1 - 1), "n"(W0 - 1) : "scc")
#define SETMODE(i) do { const int lag_ = (int)((unsigned)__builtin_amdgcn_s_memrealtime() - t00) - ((LINE[i] * (int)(LS * 1024)) >> 10); \
  wm = __builtin_amdgcn_readfirstlane((lag_ > L0) + (lag_ > L1) + (lag_ > L2) + (lag_ > L3)); } while (0)
#define GSLP 0
#define AST 16
#define ALD 16
#define NA 3
#define NS 9
#define NTHREADS ((NS + NA) * 64)

constexpr int DM = 576, NH = 32, SK = 4096, DH = 128, DF = 4608;
constexpr int NGU = 144;
constexpr int FH = DF / NH;
constexpr u64 TAG_STEP = 0x9E3779B97Full;
constexpr unsigned SPIN_CAP = 1u << 18;

__device__ __forceinline__ float dot2(unsigned a, unsigned b, float c) {
  return __builtin_amdgcn_fdot2_f32_bf16(__builtin_bit_cast(bf16x2, a), __builtin_bit_cast(bf16x2, b), c, false);
}
__device__ __forceinline__ float bflo(unsigned v) { return __builtin_bit_cast(float, v << 16); }
__device__ __forceinline__ float bfhi(unsigned v) { return __builtin_bit_cast(float, v & 0xffff0000u); }
__device__ __forceinline__ unsigned pk_lo(unsigned a, unsigned b) { return __builtin_amdgcn_perm(b, a, 0x05040100u); }
__device__ __forceinline__ unsigned pk_hi(unsigned a, unsigned b) { return __builtin_amdgcn_perm(b, a, 0x07060302u); }
__device__ __forceinline__ unsigned pack2(float a, float b) {
  return __builtin_bit_cast(unsigned, __builtin_convertvector(f32x2{a, b}, bf16x2));
}
__device__ __forceinline__ unsigned f2bf(float f) { return pack2(f, 0.f) & 0xffffu; }
template <int CTRL> __device__ __forceinline__ float dpp(float v) {
  return __builtin_bit_cast(float, __builtin_amdgcn_update_dpp(0, __builtin_bit_cast(int, v), CTRL, 0xf, 0xf, true));
}
__device__ __forceinline__ float row_sum(float v) {
  v += dpp<0x128>(v); v += dpp<0x124>(v); v += dpp<0x122>(v); v += dpp<0x121>(v);
  return v;
}
__device__ __forceinline__ float row_max(float v) {
  v = fmaxf(v, dpp<0x128>(v)); v = fmaxf(v, dpp<0x124>(v)); v = fmaxf(v, dpp<0x122>(v)); v = fmaxf(v, dpp<0x121>(v));
  return v;
}
__device__ __forceinline__ float asf(unsigned u) { return __builtin_bit_cast(float, u); }
__device__ __forceinline__ unsigned asu(float f) { return __builtin_bit_cast(unsigned, f); }
__device__ __forceinline__ float sum32(float a, float b) {
  const auto r = __builtin_amdgcn_permlane32_swap(asu(a), asu(b), false, false);
  return asf(r[0]) + asf(r[1]);
}
__device__ __forceinline__ float sum16(float a, float b) {
  const auto r = __builtin_amdgcn_permlane16_swap(asu(a), asu(b), false, false);
  return asf(r[0]) + asf(r[1]);
}
__device__ __forceinline__ float wave_sum(float v) { v = row_sum(v); v = sum16(v, v); return sum32(v, v); }
__device__ __forceinline__ float wave_max(float v) {
  v = row_max(v);
  const auto r = __builtin_amdgcn_permlane16_swap(asu(v), asu(v), false, false);
  v = fmaxf(asf(r[0]), asf(r[1]));
  const auto s = __builtin_amdgcn_permlane32_swap(asu(v), asu(v), false, false);
  return fmaxf(asf(s[0]), asf(s[1]));
}
__device__ __forceinline__ f32x4 mfma(u32x4 a, u32x4 b, f32x4 c) {
  return __builtin_amdgcn_mfma_f32_16x16x32_bf16(__builtin_bit_cast(bf16x8, a), __builtin_bit_cast(bf16x8, b), c, 0, 0, 0);
}
__device__ __forceinline__ void wg_barrier() {
  asm volatile("s_waitcnt lgkmcnt(0)" ::: "memory");
  __builtin_amdgcn_s_barrier();
  asm volatile("" ::: "memory");
}
typedef __amdgpu_buffer_rsrc_t rsrc_t;
__device__ __forceinline__ rsrc_t mk_rsrc(const void* p, unsigned bytes) { return __builtin_amdgcn_make_buffer_rsrc((void*)p, (short)0, (int)bytes, 0x00020000); }
__device__ __forceinline__ u32x4 ld16(rsrc_t r, unsigned voff, unsigned soff) { return __builtin_amdgcn_raw_buffer_load_b128(r, (int)voff, (int)soff, 2); }
__device__ __forceinline__ u32x2 ld8(rsrc_t r, unsigned voff, unsigned soff) { return __builtin_amdgcn_raw_buffer_load_b64(r, (int)voff, (int)soff, 2); }
#define SCHED_FENCE() __builtin_amdgcn_sched_barrier(0)
#define VMWAIT(n) asm volatile("s_waitcnt vmcnt(%0)" :: "n"(n))

#define TRACE(i)
#define QSTAMP(k)
#define SSTAMP(k)
#define TRACE_DUMP()
#define STAMP(k)
#define STAMP_INIT()
#define STAMP_DUMP()

__device__ __forceinline__ u64 mk_gran(u64 tag, float v) {
  const unsigned u = __builtin_bit_cast(unsigned, v);
  const unsigned r = ((u & 0x7f800000u) == 0x7f800000u) ? (u | ((u & 0xffu) ? 0x100u : 0u)) : u + 0x80u;
  return (tag << 24) | (u64)(r >> 8);
}
__device__ __forceinline__ float gran_val(u64 g) { return __builtin_bit_cast(float, (unsigned)g << 8); }
__device__ __forceinline__ void put_gran(u64* p, u64 g) { __hip_atomic_store((GAS u64*)p, g, __ATOMIC_RELAXED, __HIP_MEMORY_SCOPE_AGENT); }
__device__ __forceinline__ u64 get_gran(const u64* p) { return __hip_atomic_load((const GAS u64*)p, __ATOMIC_RELAXED, __HIP_MEMORY_SCOPE_AGENT); }
template <int SLEEP> __device__ __forceinline__ float wait_gran(const u64* p, u64 tag) {
  u64 v = get_gran(p);
  unsigned spins = 0;
#pragma nounroll
  while ((v >> 24) != tag) {
    __builtin_amdgcn_s_sleep(SLEEP);
    v = get_gran(p);
    if (++spins > SPIN_CAP) break;
  }
  return gran_val(v);
}
__device__ __forceinline__ unsigned rnd22(float v) {
  const unsigned u = __builtin_bit_cast(unsigned, v);
  return (((u & 0x7f800000u) == 0x7f800000u) ? (u | ((u & 0x3ffu) ? 0x400u : 0u)) : u + 0x200u) >> 10;
}
__device__ __forceinline__ u32x4 mk_gran4(u64 tag, f32x4 v) {
  const unsigned a = rnd22(v[0]), b = rnd22(v[1]), c = rnd22(v[2]), d = rnd22(v[3]);
  return u32x4{a | (b << 22), (b >> 10) | ((unsigned)tag << 12), c | (d << 22), (d >> 10) | ((unsigned)(tag >> 20) << 12)};
}
__device__ __forceinline__ bool gran4_ok(u32x4 g, u64 tag) { return (g[1] >> 12) == ((unsigned)tag & 0xfffffu) && (g[3] >> 12) == (unsigned)(tag >> 20); }
__device__ __forceinline__ f32x4 gran4_val(u32x4 g) {
  return f32x4{asf(g[0] << 10), asf(__builtin_amdgcn_alignbit(g[1], g[0], 22) << 10), asf(g[2] << 10), asf(__builtin_amdgcn_alignbit(g[3], g[2], 22) << 10)};
}
__device__ __forceinline__ void put_gran4(rsrc_t r, unsigned idx, u32x4 g) { __builtin_amdgcn_raw_buffer_store_b128(g, r, (int)(idx * 16), 0, 18); }
__device__ __forceinline__ u32x4 get_gran4(rsrc_t r, unsigned idx) {
  const u32x4 v = __builtin_amdgcn_raw_buffer_load_b128(r, (int)(idx * 16), 0, 18);
  asm volatile("" ::: "memory");
  return v;
}

struct __attribute__((aligned(16))) Smem {
  float qpart[NS][DH];
  unsigned qf[2][64];
  float img[NA][22][64];
  float red[NS][8][64];
  float hv[FH];
  unsigned oA[NA][68];
  u64 tag[8];
  float ml[4][2];
  float gpart[16];
  unsigned ctr[8];
};

template <int L> __device__ __forceinline__ float merge(float a, float b, int lane) {
  constexpr int CTRL[4] = {0x128, 0x141, 0x4E, 0xB1};
  const bool sel = (lane >> (3 - L)) & 1;
  const float x = sel ? b : a, y = sel ? a : b;
  return x + dpp<CTRL[L]>(y);
}
constexpr int bitrev4(int i) { return ((i & 1) << 3) | ((i & 2) << 1) | ((i & 4) >> 1) | ((i & 8) >> 3); }

struct QFrag { unsigned h[4], l[4]; };
__device__ __forceinline__ QFrag build_q(const Smem& sm, int lane) {
  QFrag q;
  const int c = lane & 15;
  f32x4 a = {0.f, 0.f, 0.f, 0.f}, bb = {0.f, 0.f, 0.f, 0.f};
#pragma unroll
  for (int s = 0; s < NS; ++s) {
    a += *(const f32x4*)&sm.qpart[s][8 * c];
    bb += *(const f32x4*)&sm.qpart[s][8 * c + 4];
    if (s % 3 == 2) { asm volatile("" : "+v"(a), "+v"(bb)); SCHED_FENCE(); }
  }
  const float sc = 0.08838834764831845f * 1.4426950408889634f;
  const float v[8] = {a[0] * sc, a[1] * sc, a[2] * sc, a[3] * sc, bb[0] * sc, bb[1] * sc, bb[2] * sc, bb[3] * sc};
#pragma unroll
  for (int d = 0; d < 4; ++d) {
    const unsigned hi = pack2(v[2 * d], v[2 * d + 1]);
    q.h[d] = hi;
    q.l[d] = pack2(v[2 * d] - bflo(hi), v[2 * d + 1] - bfhi(hi));
  }
  return q;
}
__device__ __forceinline__ float qk(const u32x4& k, const QFrag& q) {
  float d0 = dot2(k[0], q.h[0], 0.f), d1 = dot2(k[0], q.l[0], 0.f);
#pragma unroll
  for (int d = 1; d < 4; ++d) { d0 = dot2(k[d], q.h[d], d0); d1 = dot2(k[d], q.l[d], d1); }
  return d0 + d1;
}
template <int J> __device__ __forceinline__ u32x4 bfrag(const u32x2* r) {
  u32x4 f;
#pragma unroll
  for (int d = 0; d < 4; ++d) f[d] = (J & 1) ? pk_hi(r[2 * d][J >> 1], r[2 * d + 1][J >> 1]) : pk_lo(r[2 * d][J >> 1], r[2 * d + 1][J >> 1]);
  return f;
}

__global__ void __launch_bounds__(NTHREADS)
fused_decode_kernel(const u16* __restrict__ x, const u32x4* __restrict__ Kc, const u32x4* __restrict__ Vc,
                    const u16* __restrict__ wq, const u16* __restrict__ wo, const u16* __restrict__ wg,
                    const u16* __restrict__ wu, const u16* __restrict__ wd, const u16* __restrict__ gw,
                    u16* __restrict__ out, u32x4* slotA, u32x4* slotG, u64* epoch) {
  __shared__ Smem sm;
  const int t = threadIdx.x, lane = t & 63;
  const int wave = __builtin_amdgcn_readfirstlane(t >> 6);
  const int bid = blockIdx.x;
  const int h = bid & 31;
  const int b = bid >> 5;
  const size_t base = (size_t)bid * (SK * DH / 8);
  const unsigned voff = lane * 16;
  STAMP_INIT();
  const unsigned href = (unsigned)__builtin_amdgcn_s_memrealtime();

  if (wave >= NS) {
    __builtin_amdgcn_s_setprio(2);
    const int w = wave - NS;
    const rsrc_t vR = mk_rsrc(Vc + base, SK * DH * 2);
    unsigned nso = w * 8192;
    unsigned tgt = ((unsigned)__builtin_amdgcn_s_memrealtime() << 8) + (unsigned)(int)((T19 - 36 * SL) * 256);
    u32x4 r[4][8];
    const unsigned t00 = (unsigned)__builtin_amdgcn_s_memrealtime();
    unsigned wm = 1;
    unsigned hev = t00;
    {
      const rsrc_t kR = mk_rsrc(Kc + base, SK * DH * 2);
#pragma unroll
      for (int st = 0; st < 4; ++st) {
#pragma unroll
        for (int j = 0; j < 8; ++j) { VMWAIT(WINP - 1); r[st][j] = ld16(kR, voff, nso + j * 1024); SCHED_FENCE(); }
        nso += NA * 8192;
      }
    }
    STAMP(4);
    wg_barrier();
    STAMP(1);
    if (HEV == 1) hev = (unsigned)__builtin_amdgcn_s_memrealtime();
    float mx = -INFINITY;
    {
      const QFrag q = build_q(sm, lane);
      if (w == 2) {
        if (lane < 16) {
          *(u32x4*)&sm.qf[0][4 * lane] = u32x4{q.h[0], q.h[1], q.h[2], q.h[3]};
          *(u32x4*)&sm.qf[1][4 * lane] = u32x4{q.l[0], q.l[1], q.l[2], q.l[3]};
        }
        asm volatile("s_waitcnt lgkmcnt(0)" ::: "memory");
        if (lane == 0) __hip_atomic_store((LAS unsigned*)&sm.ctr[4], 1u, __ATOMIC_RELAXED, __HIP_MEMORY_SCOPE_WORKGROUP);
      }
      auto kpair = [&](u32x4 (&ra)[8], u32x4 (&rb)[8], int p) {
        const rsrc_t nR = mk_rsrc((p < 17 ? Kc : Vc) + base, SK * DH * 2);
        if (p == 17) nso = w * 8192;
        float lv[4], v;
#pragma unroll
        for (int idx = 0; idx < 16; ++idx) {
          u32x4& k = idx < 8 ? ra[idx & 7] : rb[idx & 7];
          v = qk(k, q);
          asm volatile("" : "+v"(v));
          MWAIT(); k = ld16(nR, voff, nso + (idx & 7) * 1024);
          SCHED_FENCE();
          if ((idx & 7) == 7) nso += NA * 8192;
          if (!(idx & 1)) { lv[0] = v; continue; }
          v = merge<0>(lv[0], v, lane);
          if (!(idx & 2)) { lv[1] = v; continue; }
          v = merge<1>(lv[1], v, lane);
          if (!(idx & 4)) { lv[2] = v; continue; }
          v = merge<2>(lv[2], v, lane);
          if (!(idx & 8)) { lv[3] = v; continue; }
          v = merge<3>(lv[3], v, lane);
        }
        sm.img[w][p][lane] = v;
        mx = fmaxf(mx, v);
      };
#pragma nounroll
      for (int it = 0; it < 9; ++it) {
        TRACE(it);
        SETMODE(it);
        if (TRIG < 10 && it == TRIG && t == NS * 64) __hip_atomic_store((LAS unsigned*)&sm.ctr[3], 1u, __ATOMIC_RELAXED, __HIP_MEMORY_SCOPE_WORKGROUP);
        kpair(r[0], r[1], 2 * it);
        kpair(r[2], r[3], 2 * it + 1);
      }
      TRACE(9);
      SETMODE(9);
      kpair(r[0], r[1], 18);
    }
    STAMP(2);
    if (HEV == 2) hev = (unsigned)__builtin_amdgcn_s_memrealtime();
    while (__builtin_amdgcn_readfirstlane(__hip_atomic_load((LAS unsigned*)&sm.ctr[0], __ATOMIC_RELAXED, __HIP_MEMORY_SCOPE_WORKGROUP)) < 7u)
      __builtin_amdgcn_s_sleep(1);
    asm volatile("" ::: "memory");
    float e0 = sm.img[w][20][lane], e1 = sm.img[w][21][lane];
    if ((lane & 1) || w == 2) e1 = -INFINITY;
    mx = wave_max(fmaxf(fmaxf(mx, sm.img[w][19][lane]), fmaxf(e0, e1)));
    e0 = __builtin_amdgcn_exp2f(e0 - mx);
    e1 = __builtin_amdgcn_exp2f(e1 - mx);
    float l = e0 + e1;
    f32x2 acc[4];
#pragma unroll
    for (int d = 0; d < 4; ++d) acc[d] = f32x2{0.f, 0.f};
    auto vtile = [&](u32x4 (&vt)[8], float p, auto halfc, auto modec) {
      constexpr int HALF = decltype(halfc)::value, MODE = decltype(modec)::value;
      auto piece = [&](auto jc) {
        constexpr int j = decltype(jc)::value;
        const float pb = dpp<0x150 + bitrev4(8 * HALF + j)>(p);
#pragma unroll
        for (int d = 0; d < 4; ++d) acc[d] = __builtin_elementwise_fma(f32x2{bflo(vt[j][d]), bfhi(vt[j][d])}, f32x2{pb, pb}, acc[d]);
        asm volatile("" : "+v"(acc[0]), "+v"(acc[1]), "+v"(acc[2]), "+v"(acc[3]));
        if constexpr (MODE != 0 && j == 0) PACE();
        if constexpr (MODE == 1) MWAIT();
        if constexpr (MODE == 2) asm volatile("s_cmp_ge_u32 %0, %1\n\ts_cbranch_scc1 1f\n\ts_waitcnt vmcnt(%2)\n1:" :: "s"(nso), "n"(SK * DH * 2), "n"(WIN - 1) : "scc");
        if constexpr (MODE != 0) vt[j] = ld16(vR, voff, nso + j * 1024);
        SCHED_FENCE();
      };
      piece(std::integral_constant<int, 0>{}); piece(std::integral_constant<int, 1>{}); piece(std::integral_constant<int, 2>{}); piece(std::integral_constant<int, 3>{});
      piece(std::integral_constant<int, 4>{}); piece(std::integral_constant<int, 5>{}); piece(std::integral_constant<int, 6>{}); piece(std::integral_constant<int, 7>{});
      if constexpr (MODE != 0) nso += NA * 8192;
    };
    using I0 = std::integral_constant<int, 0>; using I1 = std::integral_constant<int, 1>; using I2 = std::integral_constant<int, 2>;
    float s0 = sm.img[w][0][lane], s1 = sm.img[w][1][lane];
#pragma nounroll
    for (int it = 0; it < 9; ++it) {
      const float p0 = __builtin_amdgcn_exp2f(s0 - mx), p1 = __builtin_amdgcn_exp2f(s1 - mx);
      l += p0 + p1;
      TRACE(10 + it);
      SETMODE(10 + it);
      if (TRIG >= 10 && it == TRIG - 10 && t == NS * 64) __hip_atomic_store((LAS unsigned*)&sm.ctr[3], 1u, __ATOMIC_RELAXED, __HIP_MEMORY_SCOPE_WORKGROUP);
      s0 = sm.img[w][2 * it + 2][lane]; s1 = sm.img[w][2 * it + 3][lane];
      vtile(r[2], p0, I0{}, I1{}); vtile(r[3], p0, I1{}, I1{});
      vtile(r[0], p1, I0{}, I1{}); vtile(r[1], p1, I1{}, I1{});
    }
    {
      const float p0 = __builtin_amdgcn_exp2f(s0 - mx), p1 = __builtin_amdgcn_exp2f(s1 - mx);
      TRACE(19);
      SETMODE(19);
      l = wave_sum(l + p0 + p1);
      if (lane == 0) {
        sm.ml[w][0] = mx; sm.ml[w][1] = l;
        asm volatile("s_waitcnt lgkmcnt(0)" ::: "memory");
        __hip_atomic_fetch_add((LAS unsigned*)&sm.ctr[5], 1u, __ATOMIC_RELAXED, __HIP_MEMORY_SCOPE_WORKGROUP);
      }
      vtile(r[2], p0, I0{}, I1{}); vtile(r[3], p0, I1{}, I1{});
      vtile(r[0], p1, I0{}, I2{}); vtile(r[1], p1, I1{}, I0{});
      TRACE(20);
      vtile(r[2], e0, I0{}, I0{}); vtile(r[3], e0, I1{}, I0{});
      if (w < 2) vtile(r[0], e1, I0{}, I0{});
    }
    STAMP(3);
    if (HEV == 3) hev = (unsigned)__builtin_amdgcn_s_memrealtime();
    TRACE(21);
    {
      const float u0 = sum32(acc[0][0], acc[2][0]), u1 = sum32(acc[0][1], acc[2][1]), u2 = sum32(acc[1][0], acc[3][0]), u3 = sum32(acc[1][1], acc[3][1]);
      sm.oA[w][4 * (lane & 15) + (lane >> 4)] = pack2(sum16(u0, u2), sum16(u1, u3));
    }
    wg_barrier();
    if (lane == 0) put_gran(epoch + (8u << 20) / 8 + bid * 3 + w, (sm.tag[0] << 24) | (hev & 0xffffffu));
    STAMP_DUMP();
    TRACE_DUMP();
    return;
  }

  const int n = lane & 15, kb = lane >> 4;
  const u16* xb = x + b * DM;
  u32x4 kx[16];
  const int xt0 = wave < 6 ? 114 + 6 * (wave / 3) + wave % 3 : 126 + 2 * (wave - 6), xt1 = xt0 + (wave < 6 ? 3 : 1);
  {
    u32x4 wQ[16];
    u64 ep_v = 0;
    if (t < 8) ep_v = get_gran(epoch + t);
    const u32x4 xq0 = *(const GAS u32x4*)(xb + 64 * wave + 8 * kb), xq1 = *(const GAS u32x4*)(xb + 64 * wave + 32 + 8 * kb);
    const u16 xme = xb[t], gwme = gw[t];
    SCHED_FENCE();
    const rsrc_t rQ = mk_rsrc(wq, DM * NH * DH * 2);
    const rsrc_t kR = mk_rsrc(Kc + base, SK * DH * 2);
    const unsigned qoff = (unsigned)(8 * kb) * (NH * DH * 2) + 16 * n;
#pragma unroll
    for (int i = 0; i < 16; ++i) wQ[i] = (CONT & 1) ? ld16(rQ, voff, h * 147456 + (i * 9 + wave) * 1024) : ld16(rQ, qoff, (64 * wave + 32 * (i >> 3) + (i & 7)) * (NH * DH * 2) + DH * 2 * h);
    SCHED_FENCE();
    QSTAMP(1);
    if (t < 8) sm.ctr[t] = 0u;
    {
      const float gsum = wave_sum(bflo(xme) * bflo(gwme));
      if (lane == 0) sm.gpart[wave] = gsum;
      if (t < 8) sm.tag[t] = ((ep_v >> 24) + TAG_STEP) & 0xffffffffffull;
    }
    SCHED_FENCE();
    QSTAMP(2);
    f32x4 d[8];
#pragma unroll
    for (int blk = 0; blk < 2; ++blk) {
#pragma unroll
      for (int j = 4 * blk; j < 4 * blk + 4; ++j) { VMWAIT(14 - j); kx[j] = ld16(kR, voff, xt0 * 8192 + j * 1024); SCHED_FENCE(); }
      QSTAMP(3 + blk);
#pragma unroll
      for (int j = 0; j < 8; ++j) {
        u32x4 f;
#pragma unroll
        for (int e = 0; e < 4; ++e) {
          const unsigned lo = wQ[8 * blk + 2 * e][j >> 1], hi = wQ[8 * blk + 2 * e + 1][j >> 1];
          f[e] = (j & 1) ? pk_hi(lo, hi) : pk_lo(lo, hi);
        }
        d[j] = blk ? mfma(xq1, f, d[j]) : mfma(xq0, f, f32x4{0.f, 0.f, 0.f, 0.f});
      }
      SCHED_FENCE();
    }
    if (lane < 16) {
      *(f32x4*)&sm.qpart[wave][8 * lane] = f32x4{d[0][0], d[1][0], d[2][0], d[3][0]};
      *(f32x4*)&sm.qpart[wave][8 * lane + 4] = f32x4{d[4][0], d[5][0], d[6][0], d[7][0]};
    }
    SCHED_FENCE();
#pragma unroll
    for (int j = 0; j < 8; ++j) { kx[8 + j] = ld16(kR, voff, xt1 * 8192 + j * 1024); SCHED_FENCE(); }
  }
  QSTAMP(5);
  wg_barrier();
  SCHED_FENCE();
  auto side_arrive = [&](int k) {
    asm volatile("s_waitcnt lgkmcnt(0)" ::: "memory");
    if (lane == 0) __hip_atomic_fetch_add((LAS unsigned*)&sm.ctr[k], 1u, __ATOMIC_RELAXED, __HIP_MEMORY_SCOPE_WORKGROUP);
  };
  auto side_sync = [&](int k) {
    side_arrive(k);
    while (__builtin_amdgcn_readfirstlane(__hip_atomic_load((LAS unsigned*)&sm.ctr[k], __ATOMIC_RELAXED, __HIP_MEMORY_SCOPE_WORKGROUP)) < (unsigned)NS)
      __builtin_amdgcn_s_sleep(2);
    asm volatile("" ::: "memory");
  };
  auto lds_wait = [&](int k, unsigned v) {
    while (__builtin_amdgcn_readfirstlane(__hip_atomic_load((LAS unsigned*)&sm.ctr[k], __ATOMIC_RELAXED, __HIP_MEMORY_SCOPE_WORKGROUP)) < v)
      __builtin_amdgcn_s_sleep(16);
    asm volatile("" ::: "memory");
  };
  if (wave < 7) {
    lds_wait(4, 1u);
    QFrag q;
    {
      const u32x4 qh = *(const u32x4*)&sm.qf[0][4 * n], ql = *(const u32x4*)&sm.qf[1][4 * n];
#pragma unroll
      for (int d = 0; d < 4; ++d) { q.h[d] = qh[d]; q.l[d] = ql[d]; }
    }
    float v[16];
#pragma unroll
    for (int j = 0; j < 16; ++j) v[j] = qk(kx[j], q);
#pragma unroll
    for (int j = 0; j < 8; ++j) v[j] = merge<0>(v[2 * j], v[2 * j + 1], lane);
#pragma unroll
    for (int j = 0; j < 4; ++j) v[j] = merge<1>(v[2 * j], v[2 * j + 1], lane);
#pragma unroll
    for (int j = 0; j < 2; ++j) v[j] = merge<2>(v[2 * j], v[2 * j + 1], lane);
    const float s = merge<3>(v[0], v[1], lane);
    if (wave < 6) sm.img[wave % 3][19 + wave / 3][lane] = s;
    else sm.img[lane & 1][21][lane & ~1] = s;
    side_arrive(0);
  }
  SCHED_FENCE();
  u32x4 wo_f[4][4];
  {
    const rsrc_t rO = mk_rsrc(wo, NH * DH * DM * 2);
    const unsigned ooff = (unsigned)(8 * kb) * (DM * 2) + 8 * n;
    u32x2 wr[32];
#pragma unroll
    for (int i = 0; i < 32; ++i) { VMWAIT(OWIN - 1); wr[i] = (CONT & 2) ? ld8(rO, lane * 8, h * 147456 + (i * 9 + wave) * 512) : ld8(rO, ooff, (DH * h + 32 * (i >> 3) + (i & 7)) * (DM * 2) + 128 * wave); SCHED_FENCE(); }
#pragma unroll
    for (int blk = 0; blk < 4; ++blk) {
      wo_f[blk][0] = bfrag<0>(wr + 8 * blk); wo_f[blk][1] = bfrag<1>(wr + 8 * blk);
      wo_f[blk][2] = bfrag<2>(wr + 8 * blk); wo_f[blk][3] = bfrag<3>(wr + 8 * blk);
      asm volatile("" : "+v"(wo_f[blk][0]), "+v"(wo_f[blk][1]), "+v"(wo_f[blk][2]), "+v"(wo_f[blk][3]));
    }
  }
  SCHED_FENCE();
  SSTAMP(1);
  const rsrc_t rG = mk_rsrc(slotG, 8 * 2 * DF * 4);
  if (bid < NGU) {
    const u32x4 xg0 = *(const GAS u32x4*)(x + (n & 7) * DM + 64 * wave + 8 * kb), xg1 = *(const GAS u32x4*)(x + (n & 7) * DM + 64 * wave + 32 + 8 * kb);
    u32x2 wr[16];
    const rsrc_t rW = mk_rsrc((bid & 1) ? wu : wg, DM * DF * 2);
    const unsigned s0 = 64 * wave * (DF * 2) + 128 * (bid >> 1);
    const unsigned woff = 8 * kb * (DF * 2) + 8 * n;
#pragma unroll
    for (int i = 0; i < 16; ++i) { VMWAIT(GWIN - 1); wr[i] = (CONT & 4) ? ld8(rW, lane * 8, (bid >> 1) * 73728 + (i * 9 + wave) * 512) : ld8(rW, woff, s0 + (32 * (i >> 3) + (i & 7)) * (DF * 2)); SCHED_FENCE(); }
    const f32x4 z = {0.f, 0.f, 0.f, 0.f};
    const f32x4 d0 = mfma(xg1, bfrag<0>(wr + 8), mfma(xg0, bfrag<0>(wr), z)), d1 = mfma(xg1, bfrag<1>(wr + 8), mfma(xg0, bfrag<1>(wr), z));
    const f32x4 d2 = mfma(xg1, bfrag<2>(wr + 8), mfma(xg0, bfrag<2>(wr), z)), d3 = mfma(xg1, bfrag<3>(wr + 8), mfma(xg0, bfrag<3>(wr), z));
    if (lane < 32) {
#pragma unroll
      for (int e = 0; e < 4; ++e) *(f32x4*)&sm.red[wave][4 * kb + e][4 * n] = f32x4{d0[e], d1[e], d2[e], d3[e]};
    }
    side_arrive(1);
    if (wave < 2) {
      lds_wait(1, NS);
      f32x4 s = {0.f, 0.f, 0.f, 0.f};
#pragma unroll
      for (int r = 0; r < NS; ++r) s += *(const f32x4*)&sm.red[r][t >> 4][4 * (t & 15)];
      put_gran4(rG, ((t >> 4) * 2 + (bid & 1)) * (DF / 4) + 16 * (bid >> 1) + (t & 15), mk_gran4(sm.tag[t >> 4], s));
    }
  }
  SSTAMP(2);
  u64 tag;
  {
    const u64 tv = sm.tag[b];
    const unsigned lo = __builtin_amdgcn_readfirstlane((unsigned)tv), hi = __builtin_amdgcn_readfirstlane((unsigned)(tv >> 32));
    tag = ((u64)hi << 32) | lo;
  }
  if (wave == 0) {
    lds_wait(3, 1u);
    if (lane < FH / 4) {
      const unsigned pg = b * 2 * (DF / 4) + (FH / 4) * h + lane;
      u32x4 gg = get_gran4(rG, pg), gu = get_gran4(rG, pg + DF / 4);
      unsigned spins = 0;
#pragma nounroll
      while (!(gran4_ok(gg, tag) && gran4_ok(gu, tag))) {
        __builtin_amdgcn_s_sleep(64);
        gg = get_gran4(rG, pg); gu = get_gran4(rG, pg + DF / 4);
        if (++spins > SPIN_CAP) break;
      }
      const f32x4 g = gran4_val(gg), up = gran4_val(gu);
      f32x4 hv;
#pragma unroll
      for (int e = 0; e < 4; ++e) {
        const float inner = 0.7978845608028654f * (g[e] + 0.044715f * g[e] * g[e] * g[e]);
        hv[e] = g[e] / (1.f + __expf(-2.f * inner)) * up[e];
      }
      *(f32x4*)&sm.hv[4 * lane] = hv;
    }
    side_arrive(2);
  }
  float gate;
  {
    float z = 0.f;
#pragma unroll
    for (int s = 0; s < NS; ++s) z += sm.gpart[s];
    gate = 1.f / (1.f + __expf(-z));
  }
  lds_wait(2, 1u);
  SSTAMP(3);
  f32x4 mpart;
  {
    const rsrc_t rD = mk_rsrc(wd, DF * DM * 2);
    const unsigned doff = (unsigned)(36 * kb) * (DM * 2) + 8 * n;
    f32x2 ma = {0.f, 0.f}, mb = {0.f, 0.f};
    u32x2 wr[36];
    f32x4 hq;
#pragma unroll
    for (int i = 0; i < 36 + DWIN - 1; ++i) {
      if (i < 36) wr[i] = (CONT & 8) ? ld8(rD, lane * 8, h * 165888 + (i * 9 + wave) * 512) : ld8(rD, doff, (FH * h + i) * (DM * 2) + 128 * wave);
      SCHED_FENCE();
      if (i >= DWIN - 1) {
        const int c = i - (DWIN - 1);
        if ((c & 3) == 0) hq = *(const f32x4*)&sm.hv[36 * kb + c];
        const f32x2 hh = {hq[c & 3], hq[c & 3]};
        ma = __builtin_elementwise_fma(f32x2{bflo(wr[c][0]), bfhi(wr[c][0])}, hh, ma);
        mb = __builtin_elementwise_fma(f32x2{bflo(wr[c][1]), bfhi(wr[c][1])}, hh, mb);
        asm volatile("" : "+v"(ma), "+v"(mb));
      }
      SCHED_FENCE();
    }
    mpart = f32x4{ma[0], ma[1], mb[0], mb[1]};
#pragma unroll
    for (int e = 0; e < 4; ++e) { const float u = sum32(mpart[e], mpart[e]); mpart[e] = sum16(u, u) * (1.f - gate); }
  }
  SSTAMP(4);
  const rsrc_t rA = mk_rsrc(slotA, 256 * DM * 4);
  float f0 = 0.f, f1 = 0.f, f2 = 0.f;
  int npass = 2;
  asm volatile("" : "+s"(npass));
#pragma nounroll
  for (int pass = DRY ? 0 : 1; pass < npass; ++pass) {
    if (pass) {
      lds_wait(5, NA);
      const float m0 = sm.ml[0][0], m1 = sm.ml[1][0], m2 = sm.ml[2][0];
      const float M = fmaxf(m0, fmaxf(m1, m2));
      f0 = __builtin_amdgcn_exp2f(m0 - M); f1 = __builtin_amdgcn_exp2f(m1 - M); f2 = __builtin_amdgcn_exp2f(m2 - M);
      const float sc = gate / (f0 * sm.ml[0][1] + f1 * sm.ml[1][1] + f2 * sm.ml[2][1]);
      f0 *= sc; f1 *= sc; f2 *= sc;
      asm volatile("" : "+v"(f0), "+v"(f1), "+v"(f2));
      __builtin_amdgcn_s_setprio(3);
      wg_barrier();
    }
    {
      const unsigned* ap = &sm.oA[n < NA ? n : 0][4 * kb];
      const u32x4 a0 = *(const u32x4*)ap, a1 = *(const u32x4*)(ap + 16), a2 = *(const u32x4*)(ap + 32), a3 = *(const u32x4*)(ap + 48);
      f32x4 v;
#pragma unroll
      for (int j = 0; j < 4; ++j) {
        f32x4 d = {0.f, 0.f, 0.f, 0.f};
        d = mfma(a0, wo_f[0][j], d); d = mfma(a1, wo_f[1][j], d); d = mfma(a2, wo_f[2][j], d); d = mfma(a3, wo_f[3][j], d);
        v[j] = fmaf(d[0], f0, fmaf(d[1], f1, fmaf(d[2], f2, mpart[j])));
      }
      if (pass && lane < 16) __builtin_amdgcn_raw_buffer_store_b128(mk_gran4(tag, v), rA, (int)((bid * (DM / 4) + 16 * wave + lane) * 16), 0, AST);
    }
    SSTAMP(5);
    if (wave == 8 && h < 18) {
      if (GSLP) __builtin_amdgcn_s_sleep(GSLP);
      const unsigned g0 = ((b * NH + (lane >> 3)) * (DM / 4) + 8 * h + (lane & 7)) * 16;
      u32x4 g[4];
      bool ok[4] = {false, false, false, false};
      unsigned spins = pass ? 0u : SPIN_CAP;
#pragma nounroll
      while (true) {
#pragma unroll
        for (int k = 0; k < 4; ++k) if (!ok[k]) g[k] = __builtin_amdgcn_raw_buffer_load_b128(rA, (int)g0, k * 8 * (DM / 4) * 16, ALD);
        asm volatile("" ::: "memory");
#pragma unroll
        for (int k = 0; k < 4; ++k) ok[k] = gran4_ok(g[k], tag);
        if ((ok[0] && ok[1] && ok[2] && ok[3]) || ++spins > SPIN_CAP) break;
        __builtin_amdgcn_s_sleep(ASLP);
      }
      const f32x4 s = (gran4_val(g[0]) + gran4_val(g[1])) + (gran4_val(g[2]) + gran4_val(g[3]));
      float v = sum16(sum32(s[0], s[2]), sum32(s[1], s[3]));
      v += dpp<0x128>(v);
      if (pass && !(lane & 8)) out[b * DM + 32 * h + 4 * (lane & 7) + kb] = (u16)f2bf(v);
      if (pass && h == 0 && lane == 0) put_gran(epoch + b, tag << 24);
      STAMP(6);
    }
  }
  if (bid == 0 && wave == 8) {
    while ((unsigned)__builtin_amdgcn_s_memrealtime() - href < 13000u) __builtin_amdgcn_s_sleep(8);
    float mn = 1e9f, mxx = -1e9f, sum = 0.f;
#pragma unroll
    for (int k = 0; k < 12; ++k) {
      const u64 g = get_gran(epoch + (8u << 20) / 8 + k * 64 + lane);
      float d = (float)(((int)(((unsigned)g - href) << 8)) >> 8);
      if ((g >> 24) != tag) d = 50000.f;
      mn = fminf(mn, d); mxx = fmaxf(mxx, d); sum += d;
    }
    const float st = HSTAT == 0 ? -wave_max(-mn) : HSTAT == 1 ? wave_max(mxx) : wave_sum(sum) * (1.f / 768.f);
    const int tend = __builtin_amdgcn_readfirstlane((int)st) + 20000;
    while ((int)((unsigned)__builtin_amdgcn_s_memrealtime() - href) < tend) __builtin_amdgcn_s_sleep(1);
  }
  STAMP_DUMP();
}

extern "C" void kernel_launch(void* const* d_in, const int* in_sizes, int n_in,
                              void* d_out, int out_size, void* d_ws, size_t ws_size,
                              hipStream_t stream) {
  (void)in_sizes; (void)n_in; (void)out_size; (void)ws_size;
  u64* epoch = (u64*)d_ws;
  u32x4* slotA = (u32x4*)((char*)d_ws + 4096);
  u32x4* slotG = slotA + 256 * (DM / 4);
  fused_decode_kernel<<<dim3(256), dim3(NTHREADS), 0, stream>>>(
      (const u16*)d_in[0], (const u32x4*)d_in[1], (const u32x4*)d_in[2], (const u16*)d_in[3], (const u16*)d_in[4],
      (const u16*)d_in[6], (const u16*)d_in[7], (const u16*)d_in[8], (const u16*)d_in[9],
      (u16*)d_out, slotA, slotG, epoch);
}
```

```cpp
#include <hip/hip_runtime.h>
#include <hip/hip_bf16.h>

#define GAS __attribute__((address_space(1)))
#define LAS __attribute__((address_space(3)))
typedef unsigned u32x4 __attribute__((ext_vector_type(4)));
typedef unsigned u32x2 __attribute__((ext_vector_type(2)));
typedef float f32x4 __attribute__((ext_vector_type(4)));
typedef float f32x2 __attribute__((ext_vector_type(2)));
typedef __bf16 bf16x2 __attribute__((ext_vector_type(2)));
typedef __bf16 bf16x8 __attribute__((ext_vector_type(8)));
typedef __bf16 bf16x16 __attribute__((ext_vector_type(16)));
typedef unsigned u32x8 __attribute__((ext_vector_type(8)));
typedef unsigned short u16;
typedef unsigned long long u64;

constexpr int WIN = 6;
constexpr int W0 = 6, W1 = 7, W2 = 8, W3 = 10;
constexpr int L0 = -30, L1 = 30, L2 = 80;
constexpr int OWIN = 3, GWIN = 3, DWIN = 3;
constexpr int TO = 1100, TD = 3480;
constexpr int CP0 = 5, CP1 = 11, CP2 = 15;
constexpr int BIAS = -10;
constexpr int PD = 15;
constexpr int T0 = 790, VD = 329, HALF = 170;
constexpr u64 pack_d() {
  constexpr int d[12] = {460, 360, 390, 440, 360, 340, 330, 360, 380, 380, 340, 330};
  u64 p = 0;
  for (int i = 11; i >= 0; --i) p = (p << 5) | (u64)((d[i] - VD + 2) / 5);
  return p;
}
#define MWAIT() asm volatile("s_waitcnt vmcnt(%4)\n\ts_cmp_gt_u32 %0, 2\n\ts_cbranch_scc1 1f\n\ts_waitcnt vmcnt(%1)\n\ts_cmp_gt_u32 %0, 1\n\ts_cbranch_scc1 1f\n\t" \
  "s_waitcnt vmcnt(%2)\n\ts_cmp_gt_u32 %0, 0\n\ts_cbranch_scc1 1f\n\ts_waitcnt vmcnt(%3)\n1:" \
  :: "s"(wm), "n"(W2 - 1), "n"(W1 - 1), "n"(W0 - 1), "n"(W3 - 1) : "scc")
#define NA 3
#define NS 9
#define NTHREADS ((NS + NA) * 64)

constexpr int DM = 576, NH = 32, SK = 4096, DH = 128, DF = 4608;
constexpr int NGU = 144;
constexpr int FH = DF / NH;
constexpr u64 TAG_STEP = 0x9E3779B97Full;
constexpr unsigned SPIN_CAP = 1u << 18;
constexpr unsigned OOB = 0x7ffffff0u;

__device__ __forceinline__ float dot2(unsigned a, unsigned b, float c) {
  return __builtin_amdgcn_fdot2_f32_bf16(__builtin_bit_cast(bf16x2, a), __builtin_bit_cast(bf16x2, b), c, false);
}
__device__ __forceinline__ float bflo(unsigned v) { return __builtin_bit_cast(float, v << 16); }
__device__ __forceinline__ float bfhi(unsigned v) { return __builtin_bit_cast(float, v & 0xffff0000u); }
__device__ __forceinline__ unsigned pk_lo(unsigned a, unsigned b) { return __builtin_amdgcn_perm(b, a, 0x05040100u); }
__device__ __forceinline__ unsigned pk_hi(unsigned a, unsigned b) { return __builtin_amdgcn_perm(b, a, 0x07060302u); }
__device__ __forceinline__ unsigned pack2(float a, float b) {
  return __builtin_bit_cast(unsigned, __builtin_convertvector(f32x2{a, b}, bf16x2));
}
template <int CTRL> __device__ __forceinline__ float dpp(float v) {
  return __builtin_bit_cast(float, __builtin_amdgcn_update_dpp(0, __builtin_bit_cast(int, v), CTRL, 0xf, 0xf, true));
}
__device__ __forceinline__ float row_sum(float v) {
  v += dpp<0x128>(v); v += dpp<0x124>(v); v += dpp<0x122>(v); v += dpp<0x121>(v);
  return v;
}
__device__ __forceinline__ float row_max(float v) {
  v = fmaxf(v, dpp<0x128>(v)); v = fmaxf(v, dpp<0x124>(v)); v = fmaxf(v, dpp<0x122>(v)); v = fmaxf(v, dpp<0x121>(v));
  return v;
}
__device__ __forceinline__ float asf(unsigned u) { return __builtin_bit_cast(float, u); }
__device__ __forceinline__ unsigned asu(float f) { return __builtin_bit_cast(unsigned, f); }
__device__ __forceinline__ float sum32(float a, float b) {
  const auto r = __builtin_amdgcn_permlane32_swap(asu(a), asu(b), false, false);
  return asf(r[0]) + asf(r[1]);
}
__device__ __forceinline__ float sum16(float a, float b) {
  const auto r = __builtin_amdgcn_permlane16_swap(asu(a), asu(b), false, false);
  return asf(r[0]) + asf(r[1]);
}
__device__ __forceinline__ float wave_sum(float v) { v = row_sum(v); v = sum16(v, v); return sum32(v, v); }
__device__ __forceinline__ float wave_max(float v) {
  v = row_max(v);
  const auto r = __builtin_amdgcn_permlane16_swap(asu(v), asu(v), false, false);
  v = fmaxf(asf(r[0]), asf(r[1]));
  const auto s = __builtin_amdgcn_permlane32_swap(asu(v), asu(v), false, false);
  return fmaxf(asf(s[0]), asf(s[1]));
}
__device__ __forceinline__ f32x4 mfma(u32x4 a, u32x4 b, f32x4 c) {
  return __builtin_amdgcn_mfma_f32_16x16x32_bf16(__builtin_bit_cast(bf16x8, a), __builtin_bit_cast(bf16x8, b), c, 0, 0, 0);
}
__device__ __forceinline__ f32x4 smfma(u32x4 a, u32x4 b0, u32x4 b1, f32x4 c, unsigned idx) {
  const u32x8 b = __builtin_shufflevector(b0, b1, 0, 1, 2, 3, 4, 5, 6, 7);
  return __builtin_amdgcn_smfmac_f32_16x16x64_bf16(__builtin_bit_cast(bf16x8, a), __builtin_bit_cast(bf16x16, b), c, (int)idx, 0, 0);
}
__device__ __forceinline__ void wg_barrier() {
  asm volatile("s_waitcnt lgkmcnt(0)" ::: "memory");
  __builtin_amdgcn_s_barrier();
  asm volatile("" ::: "memory");
}
typedef __amdgpu_buffer_rsrc_t rsrc_t;
__device__ __forceinline__ rsrc_t mk_rsrc(const void* p, unsigned bytes) { return __builtin_amdgcn_make_buffer_rsrc((void*)p, (short)0, (int)bytes, 0x00020000); }
__device__ __forceinline__ u32x4 ld16(rsrc_t r, unsigned voff, unsigned soff) { return __builtin_amdgcn_raw_buffer_load_b128(r, (int)voff, (int)soff, 2); }
__device__ __forceinline__ u32x2 ld8(rsrc_t r, unsigned voff, unsigned soff) { return __builtin_amdgcn_raw_buffer_load_b64(r, (int)voff, (int)soff, 2); }
#define SCHED_FENCE() __builtin_amdgcn_sched_barrier(0)
#define VMWAIT(n) asm volatile("s_waitcnt vmcnt(%0)" :: "n"(n))

constexpr float GROUND = 1.f + 0x1.6p-15f;
__device__ __forceinline__ u32x4 tag4(u64 tag) {
  return u32x4{(unsigned)tag & 0x3ffu, (unsigned)(tag >> 10) & 0x3ffu, (unsigned)(tag >> 20) & 0x3ffu, (unsigned)(tag >> 30) & 0x3ffu};
}
__device__ __forceinline__ u32x4 mk_gran4(u32x4 t, f32x4 v) {
  return u32x4{(asu(v[0]) & ~0x3ffu) | t[0], (asu(v[1]) & ~0x3ffu) | t[1], (asu(v[2]) & ~0x3ffu) | t[2], (asu(v[3]) & ~0x3ffu) | t[3]};
}
__device__ __forceinline__ bool gran4_ok(u32x4 g, u32x4 t) { return ((((g[0] ^ t[0]) | (g[1] ^ t[1])) | ((g[2] ^ t[2]) | (g[3] ^ t[3]))) & 0x3ffu) == 0; }
__device__ __forceinline__ f32x4 gran4_val(u32x4 g) {
  return f32x4{asf(g[0] & ~0x3ffu), asf(g[1] & ~0x3ffu), asf(g[2] & ~0x3ffu), asf(g[3] & ~0x3ffu)};
}
__device__ __forceinline__ void put_gran4(rsrc_t r, unsigned idx, u32x4 g) { __builtin_amdgcn_raw_buffer_store_b128(g, r, (int)(idx * 16), 0, 18); }
__device__ __forceinline__ u32x4 get_gran4(rsrc_t r, unsigned idx) {
  const u32x4 v = __builtin_amdgcn_raw_buffer_load_b128(r, (int)(idx * 16), 0, 18);
  asm volatile("" ::: "memory");
  return v;
}

struct __attribute__((aligned(16))) Smem {
  float qpart[NS][DH];
  unsigned qf[2][64];
  float img[NA][22][64];
  float red[NS][8][64];
  float hv[FH];
  unsigned oA[NA][68];
  u64 tag[8];
  float ml[4][2];
  float gpart[16];
  unsigned ctr[8];
  u64 prog;
  unsigned org;
};

template <int L> __device__ __forceinline__ float merge(float a, float b, int lane) {
  constexpr int CTRL[4] = {0x128, 0x141, 0x4E, 0xB1};
  const bool sel = (lane >> (3 - L)) & 1;
  const float x = sel ? b : a, y = sel ? a : b;
  return x + dpp<CTRL[L]>(y);
}
constexpr int bitrev4(int i) { return ((i & 1) << 3) | ((i & 2) << 1) | ((i & 4) >> 1) | ((i & 8) >> 3); }

struct QFrag { unsigned h[4], l[4]; };
__device__ __forceinline__ QFrag build_q(const Smem& sm, int lane) {
  QFrag q;
  const int c = lane & 15;
  f32x4 a = {0.f, 0.f, 0.f, 0.f}, bb = {0.f, 0.f, 0.f, 0.f};
#pragma unroll
  for (int s = 0; s < NS; ++s) {
    a += *(const f32x4*)&sm.qpart[s][8 * c];
    bb += *(const f32x4*)&sm.qpart[s][8 * c + 4];
    if (s % 3 == 2) { asm volatile("" : "+v"(a), "+v"(bb)); SCHED_FENCE(); }
  }
  const float sc = 0.08838834764831845f * 1.4426950408889634f;
  const float v[8] = {a[0] * sc, a[1] * sc, a[2] * sc, a[3] * sc, bb[0] * sc, bb[1] * sc, bb[2] * sc, bb[3] * sc};
#pragma unroll
  for (int d = 0; d < 4; ++d) {
    const unsigned hi = pack2(v[2 * d], v[2 * d + 1]);
    q.h[d] = hi;
    q.l[d] = pack2(v[2 * d] - bflo(hi), v[2 * d + 1] - bfhi(hi));
  }
  return q;
}
__device__ __forceinline__ float qk(const u32x4& k, const QFrag& q) {
  float d0 = dot2(k[0], q.h[0], 0.f), d1 = dot2(k[0], q.l[0], 0.f);
#pragma unroll
  for (int d = 1; d < 4; ++d) { d0 = dot2(k[d], q.h[d], d0); d1 = dot2(k[d], q.l[d], d1); }
  return d0 + d1;
}
template <int J> __device__ __forceinline__ u32x4 bfrag(const u32x2* r) {
  u32x4 f;
#pragma unroll
  for (int d = 0; d < 4; ++d) f[d] = (J & 1) ? pk_hi(r[2 * d][J >> 1], r[2 * d + 1][J >> 1]) : pk_lo(r[2 * d][J >> 1], r[2 * d + 1][J >> 1]);
  return f;
}

__global__ void __launch_bounds__(NTHREADS)
fused_decode_kernel(const u16* __restrict__ x, const u32x4* __restrict__ Kc, const u32x4* __restrict__ Vc,
                    const u16* __restrict__ wq, const u16* __restrict__ wo, const u16* __restrict__ wg,
                    const u16* __restrict__ wu, const u16* __restrict__ wd, const u16* __restrict__ gw,
                    u16* __restrict__ out, u32x4* slotA, u32x4* slotG, u64* epoch, u64* board) {
  __shared__ Smem sm;
  const int lane = threadIdx.x & 63, t = threadIdx.x - NA * 64;
  const int hw = __builtin_amdgcn_readfirstlane(threadIdx.x >> 6);
  const int wave = hw < NA ? hw + NS : hw - NA;
  const int bid = blockIdx.x;
  const int h = ((bid & 7) << 2) | ((bid >> 3) & 3);
  const int b = bid >> 5;
  const size_t base = (size_t)(b * NH + h) * (SK * DH / 8);
  const unsigned voff = lane * 16;

  if (wave >= NS) {
    __builtin_amdgcn_s_setprio(2);
    const int w = wave - NS;
    const int nk = w == 2 ? 10 : 9;
    const rsrc_t vR = mk_rsrc(Vc + base, SK * DH * 2);
    unsigned nso = w * 8192;
    u32x4 r[4][8];
    if (w == 0) { sm.org = (unsigned)__builtin_amdgcn_s_memrealtime(); sm.prog = 0; }
    unsigned rel = T0, wm = 1;
    const rsrc_t kR = mk_rsrc(Kc + base, SK * DH * 2);
    auto vnext = [&]() {
      nso += nso >= 120 * 8192 ? 2 * 8192 : NA * 8192;
      if (nso >= 128 * 8192) nso = w * 8192;
    };
    {
#pragma unroll
      for (int st = 0; st < 4; ++st) {
#pragma unroll
        for (int j = 0; j < 8; ++j) { VMWAIT(WIN - 1); r[st][j] = ld16(kR, voff, nso + j * 1024); SCHED_FENCE(); }
        nso += NA * 8192;
      }
    }
    wg_barrier();
    float mx = -INFINITY;
    u64 kd = pack_d();
    asm volatile("" : "+s"(kd));
    const bool rep = w == (bid >> 5) % 3;
    auto setmode = [&](int it) {
      const unsigned org = __hip_atomic_load((LAS unsigned*)&sm.org, __ATOMIC_RELAXED, __HIP_MEMORY_SCOPE_WORKGROUP);
      const unsigned own = (unsigned)__builtin_amdgcn_s_memrealtime() - rel;
      const int lag = (int)(own - org);
      rel += VD + ((unsigned)kd & 31u) * 5u;
      kd >>= 5;
      wm = __builtin_amdgcn_readfirstlane((lag > L0) + (lag > L1) + (lag > L2));
      if (rep) __hip_atomic_store((LAS u64*)&sm.prog, ((u64)own << 32) | (unsigned)it, __ATOMIC_RELAXED, __HIP_MEMORY_SCOPE_WORKGROUP);
      SCHED_FENCE();
    };
    {
      const QFrag q = build_q(sm, lane);
      if (w == 2) {
        if (lane < 16) {
          *(u32x4*)&sm.qf[0][4 * lane] = u32x4{q.h[0], q.h[1], q.h[2], q.h[3]};
          *(u32x4*)&sm.qf[1][4 * lane] = u32x4{q.l[0], q.l[1], q.l[2], q.l[3]};
        }
        asm volatile("s_waitcnt lgkmcnt(0)" ::: "memory");
        if (lane == 0) __hip_atomic_store((LAS unsigned*)&sm.ctr[4], 1u, __ATOMIC_RELAXED, __HIP_MEMORY_SCOPE_WORKGROUP);
      }
      float* im = &sm.img[w][w == 2 ? 0 : 2][lane];
      auto kiter = [&](auto lastc, int it) {
        constexpr bool LAST = decltype(lastc)::value;
        setmode(it);
#pragma unroll
        for (int p = 0; p < 2; ++p) {
          float lv[4], v;
#pragma unroll
          for (int idx = 0; idx < 16; ++idx) {
            u32x4& k = r[2 * p + (idx >> 3)][idx & 7];
            v = qk(k, q);
            asm volatile("" : "+v"(v));
            if constexpr (!LAST) {
              MWAIT(); k = ld16(kR, voff, nso + (idx & 7) * 1024);
              if ((idx & 7) == 7) nso += NA * 8192;
            } else if (idx & 1) {
              MWAIT(); r[p][idx >> 1] = ld16(vR, voff, nso + (idx >> 1) * 1024);
              if (idx == 15) vnext();
            }
            SCHED_FENCE();
            if (!(idx & 1)) { lv[0] = v; continue; }
            v = merge<0>(lv[0], v, lane);
            if (!(idx & 2)) { lv[1] = v; continue; }
            v = merge<1>(lv[1], v, lane);
            if (!(idx & 4)) { lv[2] = v; continue; }
            v = merge<2>(lv[2], v, lane);
            if (!(idx & 8)) { lv[3] = v; continue; }
            v = merge<3>(lv[3], v, lane);
          }
          im[(2 * it + p) * 64] = v;
          mx = fmaxf(mx, v);
        }
      };
#pragma nounroll
      for (int it = 0; it < nk - 1; ++it) kiter(std::false_type{}, it);
      nso = (w < 2 ? 120 + w : w) * 8192;
      kiter(std::true_type{}, nk - 1);
      rel -= HALF;
    }
    if (w < 2) {
      while (__builtin_amdgcn_readfirstlane(__hip_atomic_load((LAS unsigned*)&sm.ctr[0], __ATOMIC_RELAXED, __HIP_MEMORY_SCOPE_WORKGROUP)) < 8u)
        __builtin_amdgcn_s_sleep(1);
      asm volatile("" ::: "memory");
      mx = fmaxf(fmaxf(mx, fmaxf(sm.img[w][0][lane], sm.img[w][1][lane])), fmaxf(sm.img[w][20][lane], sm.img[w][21][lane]));
    }
    mx = wave_max(mx);
    float l = 0.f;
    f32x2 acc[4];
#pragma unroll
    for (int d = 0; d < 4; ++d) acc[d] = f32x2{0.f, 0.f};
    auto vtile = [&](auto slotc, float p, auto refillc) {
      constexpr int SLOT = decltype(slotc)::value;
      constexpr bool REFILL = decltype(refillc)::value;
      u32x4 (&vt)[8] = r[SLOT];
      u32x4 (&nt)[8] = r[SLOT ^ 2];
      auto piece = [&](auto jc) {
        constexpr int j = decltype(jc)::value;
        if constexpr (REFILL) {
          MWAIT();
          nt[j] = ld16(vR, voff, nso + j * 1024);
          SCHED_FENCE();
        }
        const float pb = dpp<0x150 + bitrev4(8 * (SLOT & 1) + j)>(p);
#pragma unroll
        for (int d = 0; d < 4; ++d) acc[d] = __builtin_elementwise_fma(f32x2{bflo(vt[j][d]), bfhi(vt[j][d])}, f32x2{pb, pb}, acc[d]);
        asm volatile("" : "+v"(acc[0]), "+v"(acc[1]), "+v"(acc[2]), "+v"(acc[3]));
        SCHED_FENCE();
      };
      piece(std::integral_constant<int, 0>{}); piece(std::integral_constant<int, 1>{}); piece(std::integral_constant<int, 2>{}); piece(std::integral_constant<int, 3>{});
      piece(std::integral_constant<int, 4>{}); piece(std::integral_constant<int, 5>{}); piece(std::integral_constant<int, 6>{}); piece(std::integral_constant<int, 7>{});
      if constexpr (REFILL) vnext();
    };
    using I0 = std::integral_constant<int, 0>; using I1 = std::integral_constant<int, 1>; using I2 = std::integral_constant<int, 2>; using I3 = std::integral_constant<int, 3>;
    float p0, p1;
    {
      const float s0 = sm.img[w][0][lane], s1 = sm.img[w][1][lane];
      p0 = __builtin_amdgcn_exp2f(s0 - mx); p1 = __builtin_amdgcn_exp2f(s1 - mx);
    }
#pragma nounroll
    for (int it = nk; it < 19; ++it) {
      setmode(it);
      const float s0 = sm.img[w][2 * (it - nk) + 2][lane], s1 = sm.img[w][2 * (it - nk) + 3][lane];
      l += p0 + p1;
      vtile(I0{}, p0, std::true_type{});
      const float n0 = __builtin_amdgcn_exp2f(s0 - mx), n1 = __builtin_amdgcn_exp2f(s1 - mx);
      if (it == 18) {
        const float lt = wave_sum(l + n0 + n1);
        sm.ml[w][0] = mx; sm.ml[w][1] = lt;
        asm volatile("s_waitcnt lgkmcnt(0)" ::: "memory");
        __hip_atomic_store((LAS unsigned*)&sm.ctr[5 + w], 1u, __ATOMIC_RELAXED, __HIP_MEMORY_SCOPE_WORKGROUP);
      }
      vtile(I1{}, p0, std::true_type{});
      vtile(I2{}, p1, std::true_type{}); vtile(I3{}, p1, std::true_type{});
      p0 = n0; p1 = n1;
    }
    setmode(19);
    vtile(I0{}, p0, std::true_type{});
    vtile(I1{}, p0, std::true_type{});
    vtile(I2{}, p1, std::false_type{}); vtile(I3{}, p1, std::false_type{});
    {
      const float u0 = sum32(acc[0][0], acc[2][0]), u1 = sum32(acc[0][1], acc[2][1]), u2 = sum32(acc[1][0], acc[3][0]), u3 = sum32(acc[1][1], acc[3][1]);
      sm.oA[w][((lane >> 4) & 1) * 32 + 2 * (lane & 15) + (lane >> 5)] = pack2(sum16(u0, u2), sum16(u1, u3));
    }
    wg_barrier();
    return;
  }

  const int n = lane & 15, kb = lane >> 4;
  const u16* xb = x + b * DM;
  u32x4 kx[16];
  const int xt0 = (wave < 4 ? 108 + 6 * (wave >> 1) : 112 + 4 * (wave >> 1)) + (wave & 1), xt1 = xt0 + (wave < 4 ? 3 : 2);
  {
    u32x4 wQ[16];
    const u32x4 xq0 = *(const GAS u32x4*)(xb + 64 * wave + 8 * kb), xq1 = *(const GAS u32x4*)(xb + 64 * wave + 32 + 8 * kb);
    const u16 xme = xb[t], gwme = gw[t];
    SCHED_FENCE();
    const rsrc_t rQ = mk_rsrc(wq, DM * NH * DH * 2);
    const rsrc_t kR = mk_rsrc(Kc + base, wave < 8 ? SK * DH * 2 : 0);
    const unsigned qoff = (unsigned)(8 * kb) * (NH * DH * 2) + 16 * n;
#pragma unroll
    for (int i = 0; i < 16; ++i) wQ[i] = ld16(rQ, qoff, (64 * wave + 32 * (i >> 3) + (i & 7)) * (NH * DH * 2) + DH * 2 * h);
    SCHED_FENCE();
    u64 ep_v = 0;
    if (wave == 8) ep_v = *(const GAS u64*)(epoch + 16 * (lane & 7));
    SCHED_FENCE();
    if (t < 8) sm.ctr[t] = 0u;
    {
      const float gsum = wave_sum(bflo(xme) * bflo(gwme));
      if (lane == 0) sm.gpart[wave] = gsum;
    }
    SCHED_FENCE();
    f32x4 d[8];
#pragma unroll
    for (int blk = 0; blk < 2; ++blk) {
#pragma unroll
      for (int j = 4 * blk; j < 4 * blk + 4; ++j) { VMWAIT(14 - j); kx[j] = ld16(kR, voff, xt0 * 8192 + j * 1024); SCHED_FENCE(); }
#pragma unroll
      for (int j = 0; j < 8; ++j) {
        u32x4 f;
#pragma unroll
        for (int e = 0; e < 4; ++e) {
          const unsigned lo = wQ[8 * blk + 2 * e][j >> 1], hi = wQ[8 * blk + 2 * e + 1][j >> 1];
          f[e] = (j & 1) ? pk_hi(lo, hi) : pk_lo(lo, hi);
        }
        d[j] = blk ? mfma(xq1, f, d[j]) : mfma(xq0, f, f32x4{0.f, 0.f, 0.f, 0.f});
      }
      SCHED_FENCE();
    }
    if (lane < 16) {
      *(f32x4*)&sm.qpart[wave][8 * lane] = f32x4{d[0][0], d[1][0], d[2][0], d[3][0]};
      *(f32x4*)&sm.qpart[wave][8 * lane + 4] = f32x4{d[4][0], d[5][0], d[6][0], d[7][0]};
    }
    SCHED_FENCE();
    asm volatile("" : "+v"(ep_v));
    if (wave == 8 && lane < 8) sm.tag[lane] = ((ep_v >> 24) + TAG_STEP) & 0xffffffffffull;
    SCHED_FENCE();
#pragma unroll
    for (int j = 0; j < 8; ++j) { kx[8 + j] = ld16(kR, voff, xt1 * 8192 + j * 1024); SCHED_FENCE(); }
  }
  wg_barrier();
  SCHED_FENCE();
  auto side_arrive = [&](int k) {
    asm volatile("s_waitcnt lgkmcnt(0)" ::: "memory");
    if (lane == 0) __hip_atomic_fetch_add((LAS unsigned*)&sm.ctr[k], 1u, __ATOMIC_RELAXED, __HIP_MEMORY_SCOPE_WORKGROUP);
  };
  auto lds_wait = [&](int k, unsigned v) {
    while (__builtin_amdgcn_readfirstlane(__hip_atomic_load((LAS unsigned*)&sm.ctr[k], __ATOMIC_RELAXED, __HIP_MEMORY_SCOPE_WORKGROUP)) < v) {
      if (k == 2) __builtin_amdgcn_s_sleep(4); else __builtin_amdgcn_s_sleep(16);
    }
    asm volatile("" ::: "memory");
  };
  auto iter_wait = [&](unsigned v) {
    while (__builtin_amdgcn_readfirstlane(__hip_atomic_load((LAS unsigned*)&sm.prog, __ATOMIC_RELAXED, __HIP_MEMORY_SCOPE_WORKGROUP)) < v)
      __builtin_amdgcn_s_sleep(16);
    asm volatile("" ::: "memory");
  };
  auto time_wait = [&](int tm, unsigned it) {
    while ((int)((unsigned)__builtin_amdgcn_s_memrealtime() -
                 __builtin_amdgcn_readfirstlane(__hip_atomic_load((LAS unsigned*)&sm.org, __ATOMIC_RELAXED, __HIP_MEMORY_SCOPE_WORKGROUP))) < tm &&
           __builtin_amdgcn_readfirstlane(__hip_atomic_load((LAS unsigned*)&sm.prog, __ATOMIC_RELAXED, __HIP_MEMORY_SCOPE_WORKGROUP)) < it)
      __builtin_amdgcn_s_sleep(4);
    asm volatile("" ::: "memory");
  };
  if (wave < 8) {
    lds_wait(4, 1u);
    QFrag q;
    {
      const u32x4 qh = *(const u32x4*)&sm.qf[0][4 * n], ql = *(const u32x4*)&sm.qf[1][4 * n];
#pragma unroll
      for (int d = 0; d < 4; ++d) { q.h[d] = qh[d]; q.l[d] = ql[d]; }
    }
    float v[16];
#pragma unroll
    for (int j = 0; j < 16; ++j) v[j] = qk(kx[j], q);
#pragma unroll
    for (int j = 0; j < 8; ++j) v[j] = merge<0>(v[2 * j], v[2 * j + 1], lane);
#pragma unroll
    for (int j = 0; j < 4; ++j) v[j] = merge<1>(v[2 * j], v[2 * j + 1], lane);
#pragma unroll
    for (int j = 0; j < 2; ++j) v[j] = merge<2>(v[2 * j], v[2 * j + 1], lane);
    const float s = merge<3>(v[0], v[1], lane);
    sm.img[wave & 1][(wave >> 1) + (wave < 4 ? 20 : -2)][lane] = s;
    side_arrive(0);
  }
  SCHED_FENCE();
  u32x4 wo_f[4][4];
  {
    time_wait(TO, 2u);
    const rsrc_t rO = mk_rsrc(wo, NH * DH * DM * 2);
    const unsigned ooff = (unsigned)(8 * kb) * (DM * 2) + 8 * n;
    u32x2 wr[32];
#pragma unroll
    for (int i = 0; i < 32; ++i) { VMWAIT(OWIN - 1); wr[i] = ld8(rO, ooff, (DH * h + 32 * (i >> 3) + (i & 7)) * (DM * 2) + 128 * wave); SCHED_FENCE(); }
#pragma unroll
    for (int blk = 0; blk < 4; ++blk) {
      wo_f[blk][0] = bfrag<0>(wr + 8 * blk); wo_f[blk][1] = bfrag<1>(wr + 8 * blk);
      wo_f[blk][2] = bfrag<2>(wr + 8 * blk); wo_f[blk][3] = bfrag<3>(wr + 8 * blk);
      asm volatile("" : "+v"(wo_f[blk][0]), "+v"(wo_f[blk][1]), "+v"(wo_f[blk][2]), "+v"(wo_f[blk][3]));
    }
  }
  SCHED_FENCE();
  const rsrc_t rG = mk_rsrc(slotG, 8 * 2 * DF * 4);
  if (bid < NGU) {
    const u32x4 xg0 = *(const GAS u32x4*)(x + (n & 7) * DM + 64 * wave + 8 * kb), xg1 = *(const GAS u32x4*)(x + (n & 7) * DM + 64 * wave + 32 + 8 * kb);
    u32x2 wr[16];
    const int sl = 18 * (bid & 7) + (bid >> 3);
    const rsrc_t rW = mk_rsrc((sl & 1) ? wu : wg, DM * DF * 2);
    const unsigned s0 = 64 * wave * (DF * 2) + 128 * (sl >> 1);
    const unsigned woff = 8 * kb * (DF * 2) + 8 * n;
#pragma unroll
    for (int i = 0; i < 16; ++i) { VMWAIT(GWIN - 1); wr[i] = ld8(rW, woff, s0 + (32 * (i >> 3) + (i & 7)) * (DF * 2)); SCHED_FENCE(); }
    const f32x4 z = {0.f, 0.f, 0.f, 0.f};
    const f32x4 d0 = mfma(xg1, bfrag<0>(wr + 8), mfma(xg0, bfrag<0>(wr), z)), d1 = mfma(xg1, bfrag<1>(wr + 8), mfma(xg0, bfrag<1>(wr), z));
    const f32x4 d2 = mfma(xg1, bfrag<2>(wr + 8), mfma(xg0, bfrag<2>(wr), z)), d3 = mfma(xg1, bfrag<3>(wr + 8), mfma(xg0, bfrag<3>(wr), z));
    if (lane < 32) {
#pragma unroll
      for (int e = 0; e < 4; ++e) *(f32x4*)&sm.red[wave][4 * kb + e][4 * n] = f32x4{d0[e], d1[e], d2[e], d3[e]};
    }
    side_arrive(1);
    if (wave < 2) {
      lds_wait(1, NS);
      f32x4 s = {0.f, 0.f, 0.f, 0.f};
#pragma unroll
      for (int r = 0; r < NS; ++r) s += *(const f32x4*)&sm.red[r][t >> 4][4 * (t & 15)];
      put_gran4(rG, ((t >> 4) * 2 + (sl & 1)) * (DF / 4) + 16 * (sl >> 1) + (t & 15), mk_gran4(tag4(sm.tag[t >> 4]), s * GROUND));
    }
  }
  u64 tag;
  {
    const u64 tv = sm.tag[b];
    const unsigned lo = __builtin_amdgcn_readfirstlane((unsigned)tv), hi = __builtin_amdgcn_readfirstlane((unsigned)(tv >> 32));
    tag = ((u64)hi << 32) | lo;
  }
  const u32x4 tg = tag4(tag);
  auto monitor = [&](int i, unsigned c) {
    const rsrc_t rB = mk_rsrc(board, 3 * 8192);
    const u64 btag = sm.tag[0];
    iter_wait(c);
    if (((bid >> 3) & 3) == ((bid >> 5) & 3) && lane < 8) {
      const unsigned own = (unsigned)(__hip_atomic_load((LAS u64*)&sm.prog, __ATOMIC_RELAXED, __HIP_MEMORY_SCOPE_WORKGROUP) >> 32);
      const unsigned lo = ((unsigned)btag << 24) | (own & 0xffffffu), hi = (unsigned)(btag >> 8);
      __builtin_amdgcn_raw_buffer_store_b128(u32x4{lo, hi, lo, hi}, rB, lane * 16, i * 8192 + (((bid >> 5) << 3) | (bid & 7)) * 128, 16);
    }
    iter_wait(c + 1);
    const u32x2 g = __builtin_amdgcn_raw_buffer_load_b64(rB, lane * 128, i * 8192, 0);
    const unsigned org = __hip_atomic_load((LAS unsigned*)&sm.org, __ATOMIC_RELAXED, __HIP_MEMORY_SCOPE_WORKGROUP);
    const bool ok = g[1] == (unsigned)(btag >> 8) && (g[0] >> 24) == ((unsigned)btag & 0xffu);
    const int cnt = __builtin_popcountll(__builtin_amdgcn_ballot_w64(ok));
    const float sum = wave_sum(ok ? (float)((int)((g[0] - org) << 8) >> 8) : 0.f);
    if (cnt >= 24) __hip_atomic_store((LAS unsigned*)&sm.org, org + (unsigned)((int)(sum / (float)cnt) + BIAS), __ATOMIC_RELAXED, __HIP_MEMORY_SCOPE_WORKGROUP);
  };
  if (wave == 7) monitor(0, CP0);
  if (wave == 0) {
    time_wait(TD, 8u);
    if (lane < FH / 4) {
      const unsigned pg = b * 2 * (DF / 4) + (FH / 4) * h + lane;
      u32x4 gg = get_gran4(rG, pg), gu = get_gran4(rG, pg + DF / 4);
      unsigned spins = 0;
#pragma nounroll
      while (!(gran4_ok(gg, tg) && gran4_ok(gu, tg))) {
        __builtin_amdgcn_s_sleep(64);
        gg = get_gran4(rG, pg); gu = get_gran4(rG, pg + DF / 4);
        if (++spins > SPIN_CAP) break;
      }
      const f32x4 g = gran4_val(gg), up = gran4_val(gu);
      f32x4 hv;
#pragma unroll
      for (int e = 0; e < 4; ++e) {
        const float inner = 0.7978845608028654f * (g[e] + 0.044715f * g[e] * g[e] * g[e]);
        hv[e] = g[e] / (1.f + __expf(-2.f * inner)) * up[e];
      }
      *(f32x4*)&sm.hv[4 * lane] = hv;
    }
    side_arrive(2);
  }
  float gate;
  {
    float z = 0.f;
#pragma unroll
    for (int s = 0; s < NS; ++s) z += sm.gpart[s];
    gate = 1.f / (1.f + __expf(-z));
  }
  lds_wait(2, 1u);
  f32x4 mpart;
  {
    const rsrc_t rD = mk_rsrc(wd, DF * DM * 2);
    const unsigned doff = (unsigned)(36 * kb) * (DM * 2) + 8 * n;
    f32x2 ma = {0.f, 0.f}, mb = {0.f, 0.f};
    u32x2 wr[36];
    f32x4 hq;
#pragma unroll
    for (int i = 0; i < 36 + DWIN - 1; ++i) {
      if (i < 36) wr[i] = ld8(rD, doff, (FH * h + i) * (DM * 2) + 128 * wave);
      SCHED_FENCE();
      if (i >= DWIN - 1) {
        const int c = i - (DWIN - 1);
        if ((c & 3) == 0) hq = *(const f32x4*)&sm.hv[36 * kb + c];
        const f32x2 hh = {hq[c & 3], hq[c & 3]};
        ma = __builtin_elementwise_fma(f32x2{bflo(wr[c][0]), bfhi(wr[c][0])}, hh, ma);
        mb = __builtin_elementwise_fma(f32x2{bflo(wr[c][1]), bfhi(wr[c][1])}, hh, mb);
        asm volatile("" : "+v"(ma), "+v"(mb));
      }
      SCHED_FENCE();
    }
    mpart = f32x4{ma[0], ma[1], mb[0], mb[1]};
#pragma unroll
    for (int e = 0; e < 4; ++e) { const float u = sum32(mpart[e], mpart[e]); mpart[e] = sum16(u, u) * ((1.f - gate) * (0.5f * GROUND)); }
  }
  if (wave == 7) {
#pragma nounroll
    for (int i = 1; i <= 2; ++i) monitor(i, i == 1 ? CP1 : CP2);
  }
  lds_wait(5, 1u); lds_wait(6, 1u); lds_wait(7, 1u);
  {
    const float x0 = sm.ml[0][0], x1 = sm.ml[1][0], x2 = sm.ml[2][0];
    const float M = fmaxf(x0, fmaxf(x1, x2));
    float f0 = __builtin_amdgcn_exp2f(x0 - M), f1 = __builtin_amdgcn_exp2f(x1 - M), f2 = __builtin_amdgcn_exp2f(x2 - M);
    const float sc = gate * GROUND / (f0 * sm.ml[0][1] + f1 * sm.ml[1][1] + f2 * sm.ml[2][1]);
    f0 *= sc; f1 *= sc; f2 *= sc;
    u32x2 tv = (kb & 1) ? u32x2{tg[2], tg[3]} : u32x2{tg[0], tg[1]};
    unsigned ao = lane < 16 ? ((b * NH + h) * (DM / 4) + 16 * wave + lane) * 16 : OOB;
    unsigned idx = (n & 4) ? 0xeeeeeeeeu : 0x44444444u;
    const LAS u32x4* ap = (const LAS u32x4*)&sm.oA[(n & 3) < NA ? (n & 3) : 0][(n & 4) * 8 + 4 * kb];
    f32x4 d[4] = {};
    unsigned g0 = ((b * NH + (lane >> 3)) * (DM / 4) + 8 * h + (lane & 7)) * 16;
    unsigned oo = (lane & 8) ? OOB : (b * DM + 32 * h + 4 * (lane & 7) + kb) * 2;
    unsigned eo = (h | lane) ? OOB : b * 128;
    u32x2 ev = {(unsigned)tag << 24, (unsigned)(tag >> 8)};
    rsrc_t rO = mk_rsrc(out, 8 * DM * 2), rE = mk_rsrc(epoch, 1024), rA = mk_rsrc(slotA, 256 * DM * 4);
    unsigned mk = 0x3ffu, o1 = 8 * (DM / 4) * 16, o2 = 2 * o1, o3 = 3 * o1, spins = SPIN_CAP;
    u64 m0 = ~0ull, m1 = ~0ull, m2 = ~0ull, m3 = ~0ull;
    asm volatile("" : "+v"(f0), "+v"(f1), "+v"(f2), "+v"(tv), "+v"(ao), "+v"(idx), "+v"(d[0]), "+v"(d[1]), "+v"(d[2]), "+v"(d[3]));
    asm volatile("" : "+v"(g0), "+v"(oo), "+v"(eo), "+v"(ev), "+v"(ap));
    asm volatile("" : "+s"(rO), "+s"(rE), "+s"(rA), "+s"(mk), "+s"(o1), "+s"(o2), "+s"(o3), "+s"(spins), "+s"(m0), "+s"(m1), "+s"(m2), "+s"(m3));
    __builtin_amdgcn_s_setprio(3);
    wg_barrier();
    const u32x4 a0 = ap[0], a1 = ap[4];
    SCHED_FENCE();
#pragma unroll
    for (int j = 0; j < 4; ++j) d[j] = smfma(a0, wo_f[0][j], wo_f[1][j], d[j], idx);
    SCHED_FENCE();
#pragma unroll
    for (int j = 0; j < 4; ++j) d[j] = smfma(a1, wo_f[2][j], wo_f[3][j], d[j], idx);
    SCHED_FENCE();
    f32x4 v;
#pragma unroll
    for (int j = 0; j < 4; ++j) v[j] = fmaf(d[j][0], f0, fmaf(d[j][1], f1, fmaf(d[j][2], f2, mpart[j])));
    SCHED_FENCE();
    const float u0 = sum16(v[0], v[2]), u1 = sum16(v[1], v[3]);
    const unsigned w0 = __builtin_amdgcn_bitop3_b32(asu(u0), mk, tv[0], 0xba), w1 = __builtin_amdgcn_bitop3_b32(asu(u1), mk, tv[1], 0xba);
    const auto y0 = __builtin_amdgcn_permlane16_swap(w0, w0, false, false), y1 = __builtin_amdgcn_permlane16_swap(w1, w1, false, false);
    __builtin_amdgcn_raw_buffer_store_b128(u32x4{y0[0], y1[0], y0[1], y1[1]}, rA, (int)ao, 0, 16);
    SCHED_FENCE();
    if (wave == 8 && h < 18) {
      f32x4 g[4];
      unsigned tmp;
      __builtin_amdgcn_s_sleep(PD);
#define P_LD(K, SO) "s_mov_b64 exec, %[m" #K "]\n\ts_cbranch_execz 2f\n\t" \
      "buffer_load_dwordx4 v[64+4*" #K ":64+4*" #K "+3], %[g0], %[rs], " SO " offen sc1\n2:\n\t"
#define P_CHK(K) "s_mov_b64 exec, %[m" #K "]\n\ts_cbranch_execz 2f\n\t" \
      "s_waitcnt vmcnt(3-" #K ")\n\t" \
      "v_xor_b32 %[tmp], %[t0], v[64+4*" #K "]\n\t" \
      "v_bitop3_b32 %[tmp], v[64+4*" #K "+1], %[t1], %[tmp] bitop3:0xbe\n\t" \
      "v_bitop3_b32 %[tmp], v[64+4*" #K "+2], %[t2], %[tmp] bitop3:0xbe\n\t" \
      "v_bitop3_b32 %[tmp], v[64+4*" #K "+3], %[t3], %[tmp] bitop3:0xbe\n\t" \
      "v_and_b32 %[tmp], %[mk], %[tmp]\n\t" \
      "v_cmp_ne_u32 %[m" #K "], 0, %[tmp]\n\t" \
      "v_and_b32 v[64+4*" #K "], %[nm], v[64+4*" #K "]\n\t" \
      "v_and_b32 v[64+4*" #K "+1], %[nm], v[64+4*" #K "+1]\n\t" \
      "v_and_b32 v[64+4*" #K "+2], %[nm], v[64+4*" #K "+2]\n\t" \
      "v_and_b32 v[64+4*" #K "+3], %[nm], v[64+4*" #K "+3]\n2:\n\t"
      asm volatile(
        "s_cmp_eq_u32 0, 0\n1:\n\t"
        P_LD(0, "0") P_LD(1, "%[o1]") P_LD(2, "%[o2]") P_LD(3, "%[o3]")
        "s_cbranch_scc1 2f\n\ts_waitcnt vmcnt(0)\n2:\n\t"
        P_CHK(0) P_CHK(1) P_CHK(2) P_CHK(3)
        "s_or_b64 vcc, %[m0], %[m1]\n\ts_or_b64 exec, %[m2], %[m3]\n\ts_or_b64 exec, exec, vcc\n\ts_cbranch_execz 3f\n\t"
        "s_sleep 1\n\t"
        "s_sub_u32 %[sp], %[sp], 1\n\ts_cbranch_scc0 1b\n"
        "3:\n\ts_mov_b64 exec, -1\n\t"
        : [m0] "+s"(m0), [m1] "+s"(m1), [m2] "+s"(m2), [m3] "+s"(m3), [tmp] "=&v"(tmp), [sp] "+s"(spins),
          "=&{v[64:67]}"(g[0]), "=&{v[68:71]}"(g[1]), "=&{v[72:75]}"(g[2]), "=&{v[76:79]}"(g[3])
        : [g0] "v"(g0), [rs] "s"(rA), [o1] "s"(o1), [o2] "s"(o2), [o3] "s"(o3), [mk] "s"(mk), [nm] "s"(~mk),
          [t0] "s"(tg[0]), [t1] "s"(tg[1]), [t2] "s"(tg[2]), [t3] "s"(tg[3])
        : "vcc", "scc", "memory");
      const f32x4 s = (g[0] + g[1]) + (g[2] + g[3]);
      SCHED_FENCE();
      float o = sum16(sum32(s[0], s[2]), sum32(s[1], s[3]));
      o += dpp<0x128>(o);
      SCHED_FENCE();
      __builtin_amdgcn_raw_buffer_store_b16((short)pack2(o, o), rO, (int)oo, 0, 16);
      __builtin_amdgcn_raw_buffer_store_b64(ev, rE, (int)eo, 0, 16);
    }
  }
}

extern "C" void kernel_launch(void* const* d_in, const int* in_sizes, int n_in,
                              void* d_out, int out_size, void* d_ws, size_t ws_size,
                              hipStream_t stream) {
  (void)in_sizes; (void)n_in; (void)out_size; (void)ws_size;
  u64* epoch = (u64*)d_ws;
  u32x4* slotA = (u32x4*)((char*)d_ws + 4096);
  u32x4* slotG = slotA + 256 * (DM / 4);
  u64* board = (u64*)(slotG + 8 * 2 * (DF / 4));
  fused_decode_kernel<<<dim3(256), dim3(NTHREADS), 0, stream>>>(
      (const u16*)d_in[0], (const u32x4*)d_in[1], (const u32x4*)d_in[2], (const u16*)d_in[3], (const u16*)d_in[4],
      (const u16*)d_in[6], (const u16*)d_in[7], (const u16*)d_in[8], (const u16*)d_in[9],
      (u16*)d_out, slotA, slotG, epoch, board);
}
```
